```python
import torch

def _softmax(x, axis=-1):
    return torch.softmax(x, dim=axis)

def _gelu(x):
    return torch.nn.functional.gelu(x, approximate='tanh')

def _silu(x):
    return torch.nn.functional.silu(x)

def _sigmoid(x):
    return torch.sigmoid(x)

def _layernorm(x, axis=-1, eps=1e-05):
    m = x.mean(dim=axis, keepdim=True)
    v = x.var(dim=axis, keepdim=True, unbiased=False)
    return (x - m) / torch.sqrt(v + eps)

def _scatter_add(tgt, idx, src, axis=0):
    return tgt.clone().index_add_(axis, idx, src)

def _segment_sum(src, seg, n):
    out = torch.zeros((n,) + src.shape[1:], dtype=src.dtype)
    return out.index_add_(0, seg, src)

def _pad(x, pw):
    flat = tuple((v for pair in reversed(pw) for v in pair))
    return torch.nn.functional.pad(x, flat)
kernel_name = 'adaLN_cond__gqa_block_cached__swiglu_ffn__decode'

def setup_inputs():

    _w = lambda a: (lambda t: t.to(torch.bfloat16) if t.is_floating_point() else t)(torch.as_tensor(a))

    def _A_setup():
        B, Sq, D = (1, 1, 640)
        Hq, Hkv, Sk, Dh = (32, 32, 16384, 128)
        assert Hq % Hkv == 0, (Hq, Hkv)
        return {'x': _w((lambda *s: torch.randn(*s, dtype=torch.bfloat16) * 0.05)(B, Sq, D)), 'k_cache': _w((lambda *s: torch.randn(*s, dtype=torch.bfloat16) * 0.8)(B, Hkv, Sk, Dh)), 'v_cache': _w((lambda *s: torch.randn(*s, dtype=torch.bfloat16) * 1.6)(B, Hkv, Sk, Dh)), 'wq': _w((lambda *s: torch.randn(*s, dtype=torch.bfloat16) * 0.8)(D, Hq * Dh) * D ** (-0.5)), 'wo': _w((lambda *s: torch.randn(*s, dtype=torch.bfloat16) * 1.6)(Hq * Dh, D) * (Hq * Dh) ** (-0.5))}
    _A_in = _A_setup()

    def _B_setup():
        B, S, D, F = (1, 1, 640, 1728)
        return {'x': _w((lambda *s: torch.randn(*s, dtype=torch.bfloat16) * 0.05)(B, S, D)), 'w_gate': _w((lambda *s: torch.randn(*s, dtype=torch.bfloat16) * 0.05)(D, F)), 'w_up': _w((lambda *s: torch.randn(*s, dtype=torch.bfloat16) * 0.05)(D, F)), 'w_down': _w((lambda *s: torch.randn(*s, dtype=torch.bfloat16) * 0.05)(F, D))}
    _B_in = _B_setup()
    return {'A_x': _A_in['x'], 'A_k_cache': _A_in['k_cache'], 'A_v_cache': _A_in['v_cache'], 'A_wq': _A_in['wq'], 'A_wo': _A_in['wo'], 'B_x': _B_in['x'], 'B_w_gate': _B_in['w_gate'], 'B_w_up': _B_in['w_up'], 'B_w_down': _B_in['w_down'], 'c': _w((lambda *s: torch.randn(*s, dtype=torch.bfloat16) * 0.2)(1, 640) * 8.0), 'w_mod': _w((lambda *s: torch.randn(*s, dtype=torch.bfloat16) * 0.2)(640, 6 * 640) * 640 ** (-0.5) * 8.0)}

def reference(A_x, A_k_cache, A_v_cache, A_wq, A_wo, B_x, B_w_gate, B_w_up, B_w_down, c, w_mod):

    def _A_ref(x, k_cache, v_cache, wq, wo):
        B, Sq, D = x.shape
        Hq, Dh = (32, 128)
        Hkv = k_cache.shape[1]
        rep = Hq // Hkv
        q = (lambda x, *d: x.permute(*d))((x @ wq).reshape(B, Sq, Hq, Dh), 0, 2, 1, 3)
        k = (lambda x, n, axis: torch.repeat_interleave(x, n, dim=axis))(k_cache, rep, axis=1)
        v = (lambda x, n, axis: torch.repeat_interleave(x, n, dim=axis))(v_cache, rep, axis=1)
        scores = torch.einsum('bhqd,bhkd->bhqk', q, k) * Dh ** (-0.5)
        p = _softmax(scores, axis=-1)
        o = torch.einsum('bhqk,bhkd->bhqd', p, v)
        return (lambda x, *d: x.permute(*d))(o, 0, 2, 1, 3).reshape(B, Sq, Hq * Dh) @ wo

    def _B_ref(x, w_gate, w_up, w_down):
        g = _silu(x @ w_gate)
        u = x @ w_up
        return g * u @ w_down
    x = A_x
    mod = (_silu(c) @ w_mod).reshape(c.shape[0], 6, 1, -1)
    sa, ca, ga, sb, cb, gb = (mod[:, 0].clone(), mod[:, 1].clone(), mod[:, 2].clone(), mod[:, 3].clone(), mod[:, 4].clone(), mod[:, 5].clone())
    h = _layernorm(x, axis=-1) * (1 + ca) + sa
    x = x + ga * _A_ref(h, A_k_cache, A_v_cache, A_wq, A_wo)
    h = _layernorm(x, axis=-1) * (1 + cb) + sb
    x = x + gb * _B_ref(h, B_w_gate, B_w_up, B_w_down)
    return x
```

```cpp
#include <hip/hip_runtime.h>
#include <hip/hip_bf16.h>
#include <stdint.h>

typedef unsigned short u16;
typedef unsigned u32x4 __attribute__((ext_vector_type(4)));
typedef __bf16 bf16x2 __attribute__((ext_vector_type(2)));

constexpr int D = 640, HQ = 32, DH = 128, SK = 16384, FF = 1728;
constexpr int NSPLIT = 8;

__device__ __forceinline__ float bf2f(u16 b) { return __uint_as_float((unsigned)b << 16); }
__device__ __forceinline__ float rbf(float f) {
  unsigned u = __float_as_uint(f); u += 0x7FFFu + ((u >> 16) & 1u); return __uint_as_float(u & 0xffff0000u);
}
__device__ __forceinline__ float siluf(float x) { return x / (1.f + __expf(-x)); }

template <int MODE>
__global__ void k_gemv(const float* a_f32, const u16* W, int K, int N, float* out) {
  __shared__ float red[4][64];
  int col = blockIdx.x * 64 + (threadIdx.x & 63), kg = threadIdx.x >> 6;
  float acc = 0.f;
  if (col < N) for (int k = kg; k < K; k += 4) acc += a_f32[k] * bf2f(W[(size_t)k * N + col]);
  red[kg][threadIdx.x & 63] = acc;
  __syncthreads();
  if (kg == 0 && col < N) out[col] = rbf(red[0][threadIdx.x] + red[1][threadIdx.x] + red[2][threadIdx.x] + red[3][threadIdx.x]);
}

__global__ void k_silu_c(const u16* c, float* sc) { int i = threadIdx.x + blockIdx.x * blockDim.x; if (i < D) sc[i] = rbf(siluf(bf2f(c[i]))); }

__global__ void k_ln(const float* x, const float* shift, const float* scale, float* h) {
  __shared__ float red[16];
  int t = threadIdx.x;
  float v = x[t];
  float s = v;
  for (int m = 32; m >= 1; m >>= 1) s += __shfl_xor(s, m);
  if ((t & 63) == 0) red[t >> 6] = s;
  __syncthreads();
  float tot = 0; for (int i = 0; i < 10; ++i) tot += red[i];
  float mean = tot / D;
  __syncthreads();
  float dv = v - mean; s = dv * dv;
  for (int m = 32; m >= 1; m >>= 1) s += __shfl_xor(s, m);
  if ((t & 63) == 0) red[t >> 6] = s;
  __syncthreads();
  tot = 0; for (int i = 0; i < 10; ++i) tot += red[i];
  float var = rbf(tot / D);
  float meanb = rbf(mean);
  float ln = rbf(rbf(v - meanb) / rbf(sqrtf(rbf(var + 1e-5f))));
  h[t] = rbf(rbf(ln * rbf(1.f + scale[t])) + shift[t]);
}
__global__ void k_cvt(const u16* x, float* xf) { int i = threadIdx.x + blockIdx.x * blockDim.x; if (i < D) xf[i] = bf2f(x[i]); }

__global__ void __launch_bounds__(256) k_attn(const float* q, const u16* K, const u16* V, float* part) {
  __shared__ float sm[4][4], sl[4][4], so[16][128];
  int h = blockIdx.y, s = blockIdx.x, tid = threadIdx.x, lane = tid & 63, wave = tid >> 6, r = lane >> 4, c = lane & 15;
  float qf[8];
  for (int i = 0; i < 8; ++i) qf[i] = q[h * DH + c * 8 + i];
  const int per = SK / NSPLIT;
  const u16* kb = K + ((size_t)h * SK + (size_t)s * per) * DH;
  const u16* vb = V + ((size_t)h * SK + (size_t)s * per) * DH;
  float m = -INFINITY, l = 0.f, acc[8] = {0,0,0,0,0,0,0,0};
  for (int key = wave * 4 + r; key < per; key += 16) {
    u32x4 kk = *(const u32x4*)(kb + (size_t)key * DH + c * 8);
    u32x4 vv = *(const u32x4*)(vb + (size_t)key * DH + c * 8);
    float d = 0;
    for (int i = 0; i < 4; ++i) { d += qf[2*i] * __uint_as_float(kk[i] << 16); d += qf[2*i+1] * __uint_as_float(kk[i] & 0xffff0000u); }
    for (int o = 8; o >= 1; o >>= 1) d += __shfl_xor(d, o);
    float sc = d * 0.08838834764831845f;
    float mn = fmaxf(m, sc), al = __expf(m - mn), p = __expf(sc - mn);
    l = l * al + p; m = mn;
    for (int i = 0; i < 4; ++i) { acc[2*i] = acc[2*i] * al + p * __uint_as_float(vv[i] << 16); acc[2*i+1] = acc[2*i+1] * al + p * __uint_as_float(vv[i] & 0xffff0000u); }
  }
  if (c == 0) { sm[wave][r] = m; sl[wave][r] = l; }
  for (int i = 0; i < 8; ++i) so[wave * 4 + r][c * 8 + i] = acc[i];
  __syncthreads();
  if (tid < 128) {
    float M = -INFINITY; for (int i = 0; i < 16; ++i) M = fmaxf(M, sm[i >> 2][i & 3]);
    float L = 0, o = 0; for (int i = 0; i < 16; ++i) { float w = __expf(sm[i >> 2][i & 3] - M); L += w * sl[i >> 2][i & 3]; o += w * so[i][tid]; }
    float* p = part + (size_t)(h * NSPLIT + s) * 130;
    p[2 + tid] = o; if (tid == 0) { p[0] = M; p[1] = L; }
  }
}
__global__ void k_comb(const float* part, float* o) {
  int h = blockIdx.x, t = threadIdx.x;
  const float* p = part + (size_t)h * NSPLIT * 130;
  float M = -INFINITY; for (int s = 0; s < NSPLIT; ++s) M = fmaxf(M, p[s * 130]);
  float L = 0, a = 0; for (int s = 0; s < NSPLIT; ++s) { float w = __expf(p[s * 130] - M); L += w * p[s * 130 + 1]; a += w * p[s * 130 + 2 + t]; }
  o[h * DH + t] = rbf(a / L);
}
__global__ void k_res(const float* x, const float* g, const float* y, float* xo) { int t = threadIdx.x; xo[t] = rbf(x[t] + rbf(g[t] * y[t])); }
__global__ void k_act(const float* g, const float* u, float* a) { int i = threadIdx.x + blockIdx.x * blockDim.x; if (i < FF) a[i] = rbf(rbf(siluf(g[i])) * u[i]); }
__global__ void k_out(const float* x, const float* g, const float* y, u16* out) { int t = threadIdx.x; float v = rbf(x[t] + rbf(g[t] * y[t])); out[t] = (u16)(__float_as_uint(v) >> 16); }

extern "C" void kernel_launch(void* const* d_in, const int* in_sizes, int n_in, void* d_out, int out_size, void* d_ws, size_t ws_size, hipStream_t stream) {
  const u16* x = (const u16*)d_in[0]; const u16* kc = (const u16*)d_in[1]; const u16* vc = (const u16*)d_in[2];
  const u16* wq = (const u16*)d_in[3]; const u16* wo = (const u16*)d_in[4];
  const u16* wg = (const u16*)d_in[6]; const u16* wu = (const u16*)d_in[7]; const u16* wd = (const u16*)d_in[8];
  const u16* c = (const u16*)d_in[9]; const u16* wmod = (const u16*)d_in[10];
  float* ws = (float*)d_ws;
  float *sc = ws, *xf = ws + 1024, *mod = ws + 2048, *h = ws + 8192, *q = ws + 9216, *part = ws + 16384, *o = ws + 65536, *y = ws + 72000, *x2 = ws + 73000, *h2 = ws + 74000, *g = ws + 75000, *u = ws + 77000, *a = ws + 79000, *y2 = ws + 81000;
  k_silu_c<<<3, 256, 0, stream>>>(c, sc);
  k_cvt<<<3, 256, 0, stream>>>(x, xf);
  k_gemv<0><<<60, 256, 0, stream>>>(sc, wmod, D, 6 * D, mod);
  k_ln<<<1, 640, 0, stream>>>(xf, mod, mod + D, h);
  k_gemv<0><<<64, 256, 0, stream>>>(h, wq, D, HQ * DH, q);
  k_attn<<<dim3(NSPLIT, HQ), 256, 0, stream>>>(q, kc, vc, part);
  k_comb<<<HQ, 128, 0, stream>>>(part, o);
  k_gemv<0><<<10, 256, 0, stream>>>(o, wo, HQ * DH, D, y);
  k_res<<<1, 640, 0, stream>>>(xf, mod + 2 * D, y, x2);
  k_ln<<<1, 640, 0, stream>>>(x2, mod + 3 * D, mod + 4 * D, h2);
  k_gemv<0><<<27, 256, 0, stream>>>(h2, wg, D, FF, g);
  k_gemv<0><<<27, 256, 0, stream>>>(h2, wu, D, FF, u);
  k_act<<<7, 256, 0, stream>>>(g, u, a);
  k_gemv<0><<<10, 256, 0, stream>>>(a, wd, FF, D, y2);
  k_out<<<1, 640, 0, stream>>>(x2, mod + 5 * D, y2, (u16*)d_out);
}
```

```cpp
#include <hip/hip_runtime.h>
#include <hip/hip_bf16.h>
#include <stdint.h>

typedef unsigned short u16;
typedef unsigned int u32;
typedef unsigned long long u64;
typedef u32 u32x4 __attribute__((ext_vector_type(4)));
typedef u32 u32x2 __attribute__((ext_vector_type(2)));
typedef float f32x4 __attribute__((ext_vector_type(4)));
typedef float f32x2 __attribute__((ext_vector_type(2)));
typedef __bf16 bf16x8 __attribute__((ext_vector_type(8)));
typedef __bf16 bf16x2 __attribute__((ext_vector_type(2)));
typedef short s16x4 __attribute__((ext_vector_type(4)));
typedef short s16x8 __attribute__((ext_vector_type(8)));
#define GAS __attribute__((address_space(1)))
#define LAS __attribute__((address_space(3)))
#define RLX_AGENT __ATOMIC_RELAXED, __HIP_MEMORY_SCOPE_AGENT

constexpr int DM = 640, NH = 32, DH = 128, SK = 16384, FF = 1728;
constexpr u32 TAG = 0x7A5C0DE1u;
constexpr u32 SPIN_CAP = 1u << 16;

constexpr int RB = 8, RC = 4;
constexpr u32 WS_EPOCH = 0;
constexpr u32 WS_MOD = 4096;
constexpr u32 WS_CLR = WS_MOD + 8 * 3840 * 8;
constexpr u32 WS_Q = WS_CLR;
constexpr u32 WS_A0 = WS_Q + 32 * 64 * 8;
constexpr u32 WS_A = WS_A0 + 256 * 144 * 8;
constexpr u32 WS_B = WS_A + 40 * 32 * 16 * 8;
constexpr u32 WS_C = WS_B + RB * 320 * 8;
constexpr u32 WS_END = WS_C + RC * 896 * 8;
constexpr u32 CLR_CHUNKS = (WS_END - WS_CLR) / 16;
static_assert(CLR_CHUNKS <= 2 * 80 * 256, "clear coverage");

constexpr u32 L_WMOD = 0, L_WQ = 20480, L_WO = 40960, L_WF = 61440;
constexpr u32 L_SC = L_WF + 56 * 1024;
constexpr u32 L_H = L_SC + 1280;
constexpr u32 L_CB1 = L_H + 1280;
constexpr u32 L_SB = L_CB1 + 1280;
constexpr u32 L_Q = L_SB + 1280;
constexpr u32 L_RED = L_Q + 256;
constexpr u32 L_ACC = L_RED + 512;
constexpr u32 L_ML = L_ACC + 4096;
constexpr u32 L_O = L_ML + 64;
constexpr u32 L_OML = L_O + 4096;
constexpr u32 L_OB = L_OML + 64;
constexpr u32 L_X2 = L_OB + 4096;
constexpr u32 L_P = L_X2 + 1280;
constexpr u32 L_D16 = L_P + 512;
constexpr u32 L_FLAG = L_D16 + 256;
constexpr u32 LDS_BYTES = L_FLAG + 64;

__device__ __forceinline__ float bf_lo(u32 p) { return __uint_as_float(p << 16); }
__device__ __forceinline__ float bf_hi(u32 p) { return __uint_as_float(p & 0xffff0000u); }
__device__ __forceinline__ u32 pack_bf(float lo, float hi) {
  f32x2 v = {lo, hi};
  return __builtin_bit_cast(u32, __builtin_convertvector(v, bf16x2));
}
__device__ __forceinline__ float rbf(float f) { return bf_lo(pack_bf(f, f)); }
__device__ __forceinline__ float siluf(float x) { return x / (1.f + __expf(-x)); }
template <int CTRL> __device__ __forceinline__ float dpp(float x) {
  return __builtin_bit_cast(float, __builtin_amdgcn_mov_dpp(__builtin_bit_cast(int, x), CTRL, 0xf, 0xf, true));
}
template <int CTRL> __device__ __forceinline__ u32 dppu(u32 x) { return (u32)__builtin_amdgcn_mov_dpp((int)x, CTRL, 0xf, 0xf, true); }
constexpr int XOR1 = 0xB1, XOR2 = 0x4E, HMIR = 0x141, MIR = 0x140, ROR8 = 0x128, BCAST = 0x150;
__device__ __forceinline__ float xrow_sum(float x) {
  auto s = __builtin_amdgcn_permlane16_swap(__float_as_uint(x), __float_as_uint(x), false, false);
  x = __uint_as_float(s[0]) + __uint_as_float(s[1]);
  auto t = __builtin_amdgcn_permlane32_swap(__float_as_uint(x), __float_as_uint(x), false, false);
  return __uint_as_float(t[0]) + __uint_as_float(t[1]);
}
__device__ __forceinline__ float xrow_max(float x) {
  auto s = __builtin_amdgcn_permlane16_swap(__float_as_uint(x), __float_as_uint(x), false, false);
  x = fmaxf(__uint_as_float(s[0]), __uint_as_float(s[1]));
  auto t = __builtin_amdgcn_permlane32_swap(__float_as_uint(x), __float_as_uint(x), false, false);
  return fmaxf(__uint_as_float(t[0]), __uint_as_float(t[1]));
}
__device__ __forceinline__ float wave_sum(float x) {
  x += dpp<XOR1>(x); x += dpp<XOR2>(x); x += dpp<HMIR>(x); x += dpp<MIR>(x);
  return xrow_sum(x);
}
__device__ __forceinline__ float wave_max(float x) {
  x = fmaxf(x, dpp<XOR1>(x)); x = fmaxf(x, dpp<XOR2>(x)); x = fmaxf(x, dpp<HMIR>(x)); x = fmaxf(x, dpp<MIR>(x));
  return xrow_max(x);
}
__device__ __forceinline__ void lgkm0() { asm volatile("s_waitcnt lgkmcnt(0)" ::: "memory"); }
__device__ __forceinline__ void vm0() { asm volatile("s_waitcnt vmcnt(0)" ::: "memory"); }
__device__ __forceinline__ void wg_barrier() { lgkm0(); __builtin_amdgcn_s_barrier(); asm volatile("" ::: "memory"); }
__device__ __forceinline__ u64 gld(const unsigned char* ws, u32 off) { return __hip_atomic_load((GAS u64*)(ws + off), RLX_AGENT); }
__device__ __forceinline__ void gst(unsigned char* ws, u32 off, u64 v) { __hip_atomic_store((GAS u64*)(ws + off), v, RLX_AGENT); }
__device__ __forceinline__ u64 gran(u32 v) { return ((u64)TAG << 32) | v; }
__device__ __forceinline__ bool all_ok(bool ok) { return __builtin_amdgcn_ballot_w64(!ok) == 0; }

template <typename T> __device__ __forceinline__ T lds_ld(LAS unsigned char* s, u32 off) { return *(LAS T*)(s + off); }
template <typename T> __device__ __forceinline__ void lds_st(LAS unsigned char* s, u32 off, T v) { *(LAS T*)(s + off) = v; }
__device__ __forceinline__ void lds_wait(LAS unsigned char* s, u32 off, u32 target) {
  for (u32 n = 0; n < (SPIN_CAP << 4); ++n) {
    const u32 v = (u32)__builtin_amdgcn_readfirstlane((int)*(volatile LAS u32*)(s + off));
    if (v >= target) break;
    __builtin_amdgcn_s_sleep(1);
  }
  asm volatile("" ::: "memory");
}
__device__ __forceinline__ void lds_arrive(LAS unsigned char* s, u32 off, int lane) {
  lgkm0();
  if (lane == 0) __hip_atomic_fetch_add((LAS u32*)(s + off), 1u, __ATOMIC_RELAXED, __HIP_MEMORY_SCOPE_WORKGROUP);
}
__device__ __forceinline__ void dma16(const u16* g, LAS unsigned char* s, u32 off) {
  __builtin_amdgcn_global_load_lds((const GAS void*)g, (LAS void*)(s + off), 16, 0, 2);
}
__device__ __forceinline__ bf16x8 tr_frag(LAS unsigned char* s, u32 off, int lane) {
  const u32 a = off + (u32)((8 * (lane >> 4) + ((lane >> 2) & 3)) * 32 + (lane & 3) * 8);
  const s16x4 r0 = __builtin_amdgcn_ds_read_tr16_b64_v4i16((LAS s16x4*)(s + a));
  const s16x4 r1 = __builtin_amdgcn_ds_read_tr16_b64_v4i16((LAS s16x4*)(s + a + 128));
  const s16x8 r = __builtin_shufflevector(r0, r1, 0, 1, 2, 3, 4, 5, 6, 7);
  return __builtin_bit_cast(bf16x8, r);
}
__device__ __forceinline__ bf16x8 a_frag(LAS unsigned char* s, u32 off, int lane) {
  return __builtin_bit_cast(bf16x8, lds_ld<u32x4>(s, off + (u32)(lane >> 4) * 16));
}
__device__ __forceinline__ f32x4 mfma(bf16x8 a, bf16x8 b, f32x4 c) { return __builtin_amdgcn_mfma_f32_16x16x32_bf16(a, b, c, 0, 0, 0); }

struct Tile { u32x4 k[4], v[4]; };
template <int OFF> __device__ __forceinline__ void ldnt(u32x4& r, u32 voff, u64 base) {
  asm volatile("global_load_dwordx4 %0, %1, %2 offset:%3 nt" : "+v"(r) : "v"(voff), "s"(base), "i"(OFF));
}
__device__ __forceinline__ void issue(Tile& t, u32 voff, u64 kb, u64 vb) {
  ldnt<0>(t.k[0], voff, kb); ldnt<1024>(t.k[1], voff, kb); ldnt<2048>(t.k[2], voff, kb); ldnt<3072>(t.k[3], voff, kb);
  ldnt<0>(t.v[0], voff, vb); ldnt<1024>(t.v[1], voff, vb); ldnt<2048>(t.v[2], voff, vb); ldnt<3072>(t.v[3], voff, vb);
}
__device__ __forceinline__ void pin(Tile& t) {
  asm volatile("" : "+v"(t.k[0]), "+v"(t.k[1]), "+v"(t.k[2]), "+v"(t.k[3]), "+v"(t.v[0]), "+v"(t.v[1]), "+v"(t.v[2]), "+v"(t.v[3]));
}
struct Attn { float m, l; f32x4 acc; u32x4 q; u32 sel[4]; bool lo8, b4; };
__device__ __forceinline__ void wait_all(Attn& a) {
  asm volatile("s_waitcnt vmcnt(0)" : "+v"(a.acc), "+v"(a.l));
}
__device__ __forceinline__ float dot2(u32 a, u32 b, float c) {
  return __builtin_amdgcn_fdot2_f32_bf16(__builtin_bit_cast(bf16x2, a), __builtin_bit_cast(bf16x2, b), c, false);
}
__device__ __forceinline__ void consume(const Tile& t, Attn& a) {
  float s[4];
#pragma unroll
  for (int u = 0; u < 4; ++u) s[u] = dot2(t.k[u].w, a.q.w, dot2(t.k[u].z, a.q.z, dot2(t.k[u].y, a.q.y, dot2(t.k[u].x, a.q.x, 0.f))));
  const float a0 = s[0] + dpp<ROR8>(s[0]), a1 = s[1] + dpp<ROR8>(s[1]), a2 = s[2] + dpp<ROR8>(s[2]), a3 = s[3] + dpp<ROR8>(s[3]);
  const float t01 = a.lo8 ? a0 : a1, t23 = a.lo8 ? a2 : a3;
  const float wa = t01 + dpp<HMIR>(t01), wb = t23 + dpp<HMIR>(t23);
  float w = a.b4 ? wb : wa;
  w += dpp<XOR1>(w); w += dpp<XOR2>(w);
  const float sc = rbf(rbf(w) * 0.08838834764831845f);
  if (__builtin_amdgcn_ballot_w64(sc > a.m) != 0) {
    const float mx = fmaxf(a.m, wave_max(sc));
    const float f = __builtin_amdgcn_exp2f((a.m - mx) * 1.4426950408889634f);
    a.acc *= f; a.l *= f; a.m = mx;
  }
  const float p = __builtin_amdgcn_exp2f((sc - a.m) * 1.4426950408889634f);
  a.l += p;
  const float lo = p - __uint_as_float(__float_as_uint(p) & 0xffff0000u);
  const u32 pk = __builtin_amdgcn_perm(__float_as_uint(p), __float_as_uint(lo), 0x07060302u);
  const u32 pu[4] = {dppu<BCAST + 0>(pk), dppu<BCAST + 8>(pk), dppu<BCAST + 4>(pk), dppu<BCAST + 12>(pk)};
#pragma unroll
  for (int u = 0; u < 4; ++u) {
    u32x4 av;
    av.x = __builtin_amdgcn_perm(0u, pu[u], a.sel[0]); av.y = __builtin_amdgcn_perm(0u, pu[u], a.sel[1]);
    av.z = __builtin_amdgcn_perm(0u, pu[u], a.sel[2]); av.w = __builtin_amdgcn_perm(0u, pu[u], a.sel[3]);
    a.acc = mfma(__builtin_bit_cast(bf16x8, av), __builtin_bit_cast(bf16x8, t.v[u]), a.acc);
  }
}

struct LN { float meanb, den; };
__device__ __forceinline__ LN ln_stats(const u32 (&xp)[5]) {
  float s = 0.f;
#pragma unroll
  for (int i = 0; i < 5; ++i) s += bf_lo(xp[i]) + bf_hi(xp[i]);
  const float mean = wave_sum(s) * (1.f / 640.f);
  float v = 0.f;
#pragma unroll
  for (int i = 0; i < 5; ++i) { const float d0 = bf_lo(xp[i]) - mean, d1 = bf_hi(xp[i]) - mean; v += d0 * d0 + d1 * d1; }
  const float var = rbf(wave_sum(v) * (1.f / 640.f));
  LN r; r.meanb = rbf(mean); r.den = rbf(sqrtf(rbf(var + 1e-5f)));
  return r;
}
__device__ __forceinline__ float ln_mod(float x, const LN& n, float c1, float sh) {
  return rbf(rbf(rbf(rbf(x - n.meanb) / n.den) * c1) + sh);
}

#define STAMP(i) do {} while (0)
#define STAMP_FLUSH() do {} while (0)

__global__ void __launch_bounds__(512, 2) fused_decode_kernel(
    const u16* __restrict__ x, const u16* __restrict__ kc, const u16* __restrict__ vc, const u16* __restrict__ wq,
    const u16* __restrict__ wo, const u16* __restrict__ wg, const u16* __restrict__ wu, const u16* __restrict__ wd,
    const u16* __restrict__ cnd, const u16* __restrict__ wmod, u16* __restrict__ out, unsigned char* ws) {
  __shared__ __attribute__((aligned(16))) unsigned char smem_[LDS_BYTES];
  LAS unsigned char* sm = (LAS unsigned char*)smem_;
  const int tid = threadIdx.x, lane = tid & 63;
  const int wave = __builtin_amdgcn_readfirstlane(tid >> 6);
  const int xcd = blockIdx.x & 7, j = blockIdx.x >> 3;
  const int head = 4 * xcd + (j >> 3), split = j & 7;
  const bool is_d = j >= 27;
  const int dj = 5 * xcd + (j - 27), fj = 27 * xcd + j;
  STAMP(0);

  constexpr u32 F_C1 = L_FLAG, F_C2 = L_FLAG + 4, F_Q = L_FLAG + 8;

  if (__builtin_expect(wave < 4, 1)) {
    Tile t0, t1, t2, t3, t4, t5;
    const u32 voff = (u32)wave * 4096u + (u32)lane * 16u;
    const u64 kb = (u64)kc + ((u64)head * SK + (u64)split * 2048) * 256, vb = (u64)vc + ((u64)head * SK + (u64)split * 2048) * 256;
#define UNDEF(T) asm volatile("" : "=v"(T.k[0]), "=v"(T.k[1]), "=v"(T.k[2]), "=v"(T.k[3]), "=v"(T.v[0]), "=v"(T.v[1]), "=v"(T.v[2]), "=v"(T.v[3]))
    UNDEF(t0);
    issue(t0, voff, kb, vb);
    if (tid < 16) lds_st<u32>(sm, L_FLAG + tid * 4, 0u);
    wg_barrier();
    Attn a;
    a.m = -INFINITY; a.l = 0.f; a.acc = f32x4{0.f, 0.f, 0.f, 0.f};
    {
      const int i = lane & 15;
      a.lo8 = i < 8; a.b4 = (i & 4) != 0;
      const u32 src = i < 8 ? 0x0302u : 0x0100u;
      const u32 one = (i & 1) ? ((src << 16) | 0x0c0cu) : (0x0c0c0000u | src);
#pragma unroll
      for (int r = 0; r < 4; ++r) a.sel[r] = (((i & 7) >> 1) == r) ? one : 0x0c0c0c0cu;
    }
    UNDEF(t1); UNDEF(t2); UNDEF(t3); UNDEF(t4); UNDEF(t5);
    constexpr u64 TS = 16384;
    wait_all(a); issue(t1, voff, kb + 1 * TS, vb + 1 * TS); pin(t0);
    wait_all(a); issue(t2, voff, kb + 2 * TS, vb + 2 * TS); pin(t1);
    wait_all(a); issue(t3, voff, kb + 3 * TS, vb + 3 * TS); pin(t2);
    wait_all(a); issue(t4, voff, kb + 4 * TS, vb + 4 * TS); pin(t3);
    wait_all(a); issue(t5, voff, kb + 5 * TS, vb + 5 * TS); pin(t4);
    STAMP(1);
    lds_wait(sm, F_Q, 1);
    STAMP(2);
    a.q = lds_ld<u32x4>(sm, L_Q + (lane & 15) * 16);
    consume(t0, a);
    wait_all(a); issue(t0, voff, kb + 6 * TS, vb + 6 * TS); pin(t5);
    consume(t1, a); consume(t2, a); consume(t3, a);
    wait_all(a); issue(t1, voff, kb + 7 * TS, vb + 7 * TS); pin(t0);
    consume(t4, a); consume(t5, a); consume(t0, a);
    STAMP(3);
    for (int t = 8; t < 32; t += 2) {
      wait_all(a); issue(t0, voff, kb + (u64)t * TS, vb + (u64)t * TS); pin(t1);
      consume(t1, a);
      wait_all(a); issue(t1, voff, kb + (u64)(t + 1) * TS, vb + (u64)(t + 1) * TS); pin(t0);
      consume(t0, a);
    }
    wait_all(a); pin(t1);
    STAMP(4);
    consume(t1, a);
    const float lw = wave_sum(a.l) * 0.25f;
    lds_st<f32x4>(sm, L_ACC + wave * 1024 + lane * 16, a.acc);
    if (lane == 0) lds_st<f32x2>(sm, L_ML + wave * 8, f32x2{a.m, lw});
  } else {
    const int hw = wave - 4;
    wg_barrier();
    const int ll = lane < 40 ? lane : 39;
    const int k0 = 160 * hw + 4 * ll;
    const bool is_mod = j < 30, crit = j < 10;
    const int mblk = crit ? 10 * xcd + j : 80 + 20 * xcd + (j - 10);
    const u64 epoch = gld(ws, WS_EPOCH);
    const u32x2 cv = *(const GAS u32x2*)(cnd + k0);
    const u32x2 xv = *(const GAS u32x2*)(x + k0);
    u32 xp[5];
#pragma unroll
    for (int i = 0; i < 5; ++i) xp[i] = ((const GAS u32*)x)[lane + 64 * i];
    if (crit) {
      const u32x4 z = {0u, 0u, 0u, 0u};
#pragma unroll
      for (int i = 0; i < 2; ++i) {
        const u32 c = (u32)(i * 20480 + (10 * xcd + j) * 256 + hw * 64 + lane);
        if (c < CLR_CHUNKS) asm volatile("global_store_dwordx4 %0, %1, off sc1" :: "v"(ws + WS_CLR + (u64)c * 16), "v"(z) : "memory");
      }
    }
    const int r2 = lane >> 1, c8 = (lane & 1) * 8;
    if (is_mod) {
#pragma unroll
      for (int i = 0; i < 5; ++i) dma16(wmod + (size_t)(160 * hw + 32 * i + r2) * 3840 + mblk * 16 + c8, sm, L_WMOD + (hw * 5 + i) * 1024);
    }
#pragma unroll
    for (int i = 0; i < 5; ++i) dma16(wq + (size_t)(160 * hw + 32 * i + r2) * 4096 + head * 128 + split * 16 + c8, sm, L_WQ + (hw * 5 + i) * 1024);
    const u64 tag48 = ((epoch + 1) & 0xffffffffffffull) << 16;
    const LN n1 = ln_stats(xp);
    if (is_mod) {
      lds_st<u32x2>(sm, L_SC + k0 * 2, u32x2{pack_bf(siluf(bf_lo(cv.x)), siluf(bf_hi(cv.x))), pack_bf(siluf(bf_lo(cv.y)), siluf(bf_hi(cv.y)))});
      vm0(); lgkm0();
      f32x4 acc = {0.f, 0.f, 0.f, 0.f};
#pragma unroll
      for (int i = 0; i < 5; ++i) acc = mfma(a_frag(sm, L_SC + (160 * hw + 32 * i) * 2, lane), tr_frag(sm, L_WMOD + (hw * 5 + i) * 1024, lane), acc);
      if (lane < 16) lds_st<float>(sm, L_RED + (hw * 16 + lane) * 4, acc.x);
      lds_arrive(sm, F_C1, lane);
      if (hw == 0) {
        lds_wait(sm, F_C1, 4);
        const int col = lane & 15;
        const float v = lds_ld<float>(sm, L_RED + col * 4) + lds_ld<float>(sm, L_RED + (16 + col) * 4) + lds_ld<float>(sm, L_RED + (32 + col) * 4) + lds_ld<float>(sm, L_RED + (48 + col) * 4);
        const u64 g = tag48 | (pack_bf(v, v) & 0xffffu);
        const u32 o = WS_MOD + (u32)(((lane >> 4) * 3840 + mblk * 16 + col) * 8);
        gst(ws, o, g); gst(ws, o + 4 * 3840 * 8, g);
      }
    } else {
      vm0();
    }
    STAMP(1);
    bf16x8 bq[5];
#pragma unroll
    for (int i = 0; i < 5; ++i) bq[i] = tr_frag(sm, L_WQ + (hw * 5 + i) * 1024, lane);
    const u32 mrep = WS_MOD + (u32)(xcd * 3840 * 8);
    {
      u32 sa[4], ca[4];
      for (u32 n = 0; n < SPIN_CAP; ++n) {
        bool ok = true;
#pragma unroll
        for (int e = 0; e < 4; ++e) {
          const u64 g0 = gld(ws, mrep + (u32)(k0 + e) * 8), g1 = gld(ws, mrep + (u32)(640 + k0 + e) * 8);
          sa[e] = (u32)g0 << 16; ca[e] = (u32)g1 << 16;
          ok &= ((g0 ^ tag48) >> 16) == 0 && ((g1 ^ tag48) >> 16) == 0;
        }
        if (all_ok(ok)) break;
      }
      STAMP(2);
      float h[4];
      const float xe[4] = {bf_lo(xv.x), bf_hi(xv.x), bf_lo(xv.y), bf_hi(xv.y)};
#pragma unroll
      for (int e = 0; e < 4; ++e) h[e] = ln_mod(xe[e], n1, rbf(1.f + __uint_as_float(ca[e])), __uint_as_float(sa[e]));
      lds_st<u32x2>(sm, L_H + k0 * 2, u32x2{pack_bf(h[0], h[1]), pack_bf(h[2], h[3])});
      lgkm0();
      f32x4 acc = {0.f, 0.f, 0.f, 0.f};
#pragma unroll
      for (int i = 0; i < 5; ++i) acc = mfma(a_frag(sm, L_H + (160 * hw + 32 * i) * 2, lane), bq[i], acc);
      if (lane < 16) lds_st<float>(sm, L_RED + 256 + (hw * 16 + lane) * 4, acc.x);
      lds_arrive(sm, F_C2, lane);
    }
    if (hw == 0) {
      lds_wait(sm, F_C2, 4);
      const int col = lane & 15;
      const float v = lds_ld<float>(sm, L_RED + 256 + col * 4) + lds_ld<float>(sm, L_RED + 256 + (16 + col) * 4) + lds_ld<float>(sm, L_RED + 256 + (32 + col) * 4) + lds_ld<float>(sm, L_RED + 256 + (48 + col) * 4);
      const u32 pr = pack_bf(v, dpp<XOR1>(v));
      if (lane < 16 && !(lane & 1)) gst(ws, WS_Q + (u32)(head * 64 + split * 8 + (lane >> 1)) * 8, gran(pr));
      STAMP(3);
      u64 g = 0;
      for (u32 n = 0; n < SPIN_CAP; ++n) {
        g = gld(ws, WS_Q + (u32)(head * 64 + lane) * 8);
        if (all_ok((u32)(g >> 32) == TAG)) break;
      }
      lds_st<u32>(sm, L_Q + lane * 4, (u32)g);
      lgkm0();
      if (lane == 0) *(volatile LAS u32*)(sm + F_Q) = 1u;
      STAMP(4);
    }
    if (!is_d) {
      u32 sb[4], cb[4];
      for (u32 n = 0; n < SPIN_CAP; ++n) {
        bool ok = true;
#pragma unroll
        for (int e = 0; e < 4; ++e) {
          const u64 g0 = gld(ws, mrep + (u32)(1920 + k0 + e) * 8), g1 = gld(ws, mrep + (u32)(2560 + k0 + e) * 8);
          sb[e] = (u32)g0 & 0xffffu; cb[e] = (u32)g1 << 16;
          ok &= ((g0 ^ tag48) >> 16) == 0 && ((g1 ^ tag48) >> 16) == 0;
        }
        if (all_ok(ok)) break;
      }
      lds_st<u32x2>(sm, L_SB + k0 * 2, u32x2{sb[0] | (sb[1] << 16), sb[2] | (sb[3] << 16)});
      lds_st<u32x2>(sm, L_CB1 + k0 * 2, u32x2{pack_bf(1.f + __uint_as_float(cb[0]), 1.f + __uint_as_float(cb[1])), pack_bf(1.f + __uint_as_float(cb[2]), 1.f + __uint_as_float(cb[3]))});
    } else if (hw == 1) {
      const int col = lane & 15;
      const u32 o = mrep + (u32)(((lane & 16) ? 3200 : 1280) + dj * 16 + col) * 8;
      u64 g = 0;
      for (u32 n = 0; n < SPIN_CAP; ++n) {
        g = gld(ws, o);
        if (all_ok(((g ^ tag48) >> 16) == 0)) break;
      }
      if (lane < 32) lds_st<float>(sm, L_D16 + 64 + lane * 4, __uint_as_float((u32)g << 16));
      if (lane < 16) lds_st<float>(sm, L_D16 + lane * 4, __uint_as_float((u32)((const GAS u16*)x)[dj * 16 + col] << 16));
    }
#pragma unroll
    for (int i = 0; i < 5; ++i) {
      const int id = 5 * hw + i;
      dma16(wo + (size_t)(head * 128 + 32 * (id & 3) + r2) * 640 + split * 80 + (id >> 2) * 16 + c8, sm, L_WO + id * 1024);
    }
    if (!is_d) {
      const u16* wsrc = ((lane & 1) ? wu : wg) + fj * 8;
#pragma unroll
      for (int i = 0; i < 5; ++i) dma16(wsrc + (size_t)(160 * hw + 32 * i + r2) * FF, sm, L_WF + (5 * hw + i) * 1024);
    } else {
#pragma unroll
      for (int i = 0; i < 14; ++i) {
        const int s = 14 * hw + i;
        if (s < 54) dma16(wd + (size_t)(32 * s + r2) * 640 + dj * 16 + c8, sm, L_WF + s * 1024);
      }
    }
    vm0();
    STAMP(5);
  }

  wg_barrier();
  STAMP(6);
  const u32 a0blk = WS_A0 + (u32)(head * 8) * 144 * 8;
  if (wave < 3) {
    const f32x2 p0 = lds_ld<f32x2>(sm, L_ML), p1 = lds_ld<f32x2>(sm, L_ML + 8), p2 = lds_ld<f32x2>(sm, L_ML + 16), p3 = lds_ld<f32x2>(sm, L_ML + 24);
    const float M = fmaxf(fmaxf(p0.x, p1.x), fmaxf(p2.x, p3.x));
    const float e0 = __expf(p0.x - M), e1 = __expf(p1.x - M), e2 = __expf(p2.x - M), e3 = __expf(p3.x - M);
    if (wave < 2) {
      const int d = tid, c = d >> 3, i = d & 7;
      const u32 o = L_ACC + (u32)(((i >> 2) * 16 + c) * 4 + (i & 3)) * 4;
      const float v = e0 * (lds_ld<float>(sm, o) + lds_ld<float>(sm, o + 512)) + e1 * (lds_ld<float>(sm, o + 1024) + lds_ld<float>(sm, o + 1536))
                    + e2 * (lds_ld<float>(sm, o + 2048) + lds_ld<float>(sm, o + 2560)) + e3 * (lds_ld<float>(sm, o + 3072) + lds_ld<float>(sm, o + 3584));
      gst(ws, a0blk + (u32)(split * 144 + d) * 8, gran(__float_as_uint(v)));
    } else if (lane < 2) {
      const float L = e0 * p0.y + e1 * p1.y + e2 * p2.y + e3 * p3.y;
      gst(ws, a0blk + (u32)(split * 144 + 128 + lane) * 8, gran(__float_as_uint(lane ? L : M)));
    }
  }
  bf16x8 bo[4];
  if (wave < 5) {
#pragma unroll
    for (int t = 0; t < 4; ++t) bo[t] = tr_frag(sm, L_WO + (wave * 4 + t) * 1024, lane);
  }
  {
    const u32 src = a0blk + (u32)(wave * 144) * 8;
    u64 g0 = 0, g1 = 0, g2 = 0;
    for (u32 n = 0; n < SPIN_CAP; ++n) {
      g0 = gld(ws, src + (u32)lane * 8); g1 = gld(ws, src + (u32)(64 + lane) * 8); g2 = gld(ws, src + (u32)(128 + (lane & 1)) * 8);
      if (all_ok((u32)(g0 >> 32) == TAG && (u32)(g1 >> 32) == TAG && (u32)(g2 >> 32) == TAG)) break;
    }
    lds_st<u32>(sm, L_O + (wave * 128 + lane) * 4, (u32)g0);
    lds_st<u32>(sm, L_O + (wave * 128 + 64 + lane) * 4, (u32)g1);
    if (lane < 2) lds_st<u32>(sm, L_OML + (wave * 2 + lane) * 4, (u32)g2);
  }
  STAMP(7);
  wg_barrier();
  const u32 stage = L_OB + (u32)wave * 512;
  if (wave < 5) {
    const f32x2 ml = lds_ld<f32x2>(sm, L_OML + (lane & 7) * 8);
    float M = ml.x;
    M = fmaxf(M, dpp<XOR1>(M)); M = fmaxf(M, dpp<XOR2>(M)); M = fmaxf(M, dpp<HMIR>(M));
    const float e = __expf(ml.x - M);
    float L = e * ml.y;
    L += dpp<XOR1>(L); L += dpp<XOR2>(L); L += dpp<HMIR>(L);
    const float wn = e / L;
    float o0 = 0.f, o1 = 0.f;
#pragma unroll
    for (int s = 0; s < 8; ++s) {
      const float ws_ = __uint_as_float((u32)__builtin_amdgcn_readlane((int)__float_as_uint(wn), s));
      const f32x2 v = lds_ld<f32x2>(sm, L_O + (s * 128 + 2 * lane) * 4);
      o0 += ws_ * v.x; o1 += ws_ * v.y;
    }
    lds_st<u32>(sm, stage + lane * 4, pack_bf(o0, o1));
    lgkm0();
    f32x4 acc = {0.f, 0.f, 0.f, 0.f};
#pragma unroll
    for (int t = 0; t < 4; ++t) acc = mfma(a_frag(sm, stage + t * 64, lane), bo[t], acc);
    if (lane < 16) gst(ws, WS_A + (u32)(((split * 5 + wave) * 32 + head) * 16 + lane) * 8, gran(__float_as_uint(acc.x)));
  }
  STAMP(8);
  if (!is_d) {
    if (wave < 5) {
      bf16x8 bf[4];
#pragma unroll
      for (int t = 0; t < 4; ++t) bf[t] = tr_frag(sm, L_WF + (wave * 4 + t) * 1024, lane);
      const u32 cb1 = lds_ld<u32>(sm, L_CB1 + (wave * 64 + lane) * 4), sb = lds_ld<u32>(sm, L_SB + (wave * 64 + lane) * 4);
      u64 g = 0;
      const u32 src = WS_B + (u32)(xcd * 320 + wave * 64 + lane) * 8;
      for (u32 n = 0; n < SPIN_CAP; ++n) {
        g = gld(ws, src);
        if (all_ok((u32)(g >> 32) == TAG)) break;
      }
      lds_st<u32>(sm, L_X2 + (wave * 64 + lane) * 4, (u32)g);
      STAMP(9);
      wg_barrier();
      u32 xp[5];
#pragma unroll
      for (int i = 0; i < 5; ++i) xp[i] = lds_ld<u32>(sm, L_X2 + (i * 64 + lane) * 4);
      const LN n2 = ln_stats(xp);
      const u32 own = (u32)g;
      lds_st<u32>(sm, stage + lane * 4, pack_bf(ln_mod(bf_lo(own), n2, bf_lo(cb1), bf_lo(sb)), ln_mod(bf_hi(own), n2, bf_hi(cb1), bf_hi(sb))));
      lgkm0();
      f32x4 acc = {0.f, 0.f, 0.f, 0.f};
#pragma unroll
      for (int t = 0; t < 4; ++t) acc = mfma(a_frag(sm, stage + t * 64, lane), bf[t], acc);
      if (lane < 16) lds_st<float>(sm, L_P + (wave * 16 + lane) * 4, acc.x);
    } else {
      wg_barrier();
    }
    wg_barrier();
    if (wave == 0) {
      const int col = lane & 15;
      float v = 0.f;
#pragma unroll
      for (int w = 0; w < 5; ++w) v += lds_ld<float>(sm, L_P + (w * 16 + col) * 4);
      v = rbf(v);
      const float u = dpp<ROR8>(v);
      const float act = rbf(rbf(siluf(v)) * u);
      const u32 pr = pack_bf(act, dpp<XOR1>(act));
      if (col < 8 && !(col & 1)) gst(ws, WS_C + (u32)((lane >> 4) * 896 + fj * 4 + (col >> 1)) * 8, gran(pr));
    }
    STAMP(10);
  } else {
    if (wave == 7) {
      const u32 src = WS_A + (u32)((dj * 32 + (lane >> 4)) * 16 + (lane & 15)) * 8;
      u64 g[8];
      for (u32 n = 0; n < SPIN_CAP; ++n) {
        bool ok = true;
#pragma unroll
        for (int i = 0; i < 8; ++i) { g[i] = gld(ws, src + (u32)i * 4 * 16 * 8); ok &= (u32)(g[i] >> 32) == TAG; }
        if (all_ok(ok)) break;
      }
      float y = 0.f;
#pragma unroll
      for (int i = 0; i < 8; ++i) y += __uint_as_float((u32)g[i]);
      y = rbf(xrow_sum(y));
      const int col = lane & 15;
      const float x2 = rbf(lds_ld<float>(sm, L_D16 + col * 4) + rbf(lds_ld<float>(sm, L_D16 + 64 + col * 4) * y));
      const u32 pr = pack_bf(x2, dpp<XOR1>(x2));
      if (!(col & 1)) {
        const u32 o = WS_B + (u32)((lane >> 4) * 320 + dj * 8 + (col >> 1)) * 8;
        gst(ws, o, gran(pr)); gst(ws, o + 4 * 320 * 8, gran(pr));
      }
      if (lane < 16) lds_st<float>(sm, L_D16 + 192 + lane * 4, x2);
      STAMP(9);
    }
    const int nst = wave == 7 ? 5 : 7;
    bf16x8 bd[7];
#pragma unroll
    for (int t = 0; t < 7; ++t) bd[t] = tr_frag(sm, L_WF + (wave * 7 + (t < nst ? t : 0)) * 1024, lane);
    wg_barrier();
    {
      const u32 src = WS_C + (u32)((dj & 3) * 896 + wave * 112) * 8;
      const int ng = nst * 16;
      const int i1 = lane + 64 < ng ? lane + 64 : lane;
      u64 g0 = 0, g1 = 0;
      for (u32 n = 0; n < SPIN_CAP; ++n) {
        g0 = gld(ws, src + (u32)lane * 8); g1 = gld(ws, src + (u32)i1 * 8);
        if (all_ok((u32)(g0 >> 32) == TAG && (u32)(g1 >> 32) == TAG)) break;
      }
      STAMP(10);
      lds_st<u32>(sm, stage + lane * 4, (u32)g0);
      lds_st<u32>(sm, stage + i1 * 4, (u32)g1);
      lgkm0();
      f32x4 acc = {0.f, 0.f, 0.f, 0.f};
#pragma unroll
      for (int t = 0; t < 7; ++t) if (t < nst) acc = mfma(a_frag(sm, stage + t * 64, lane), bd[t], acc);
      if (lane < 16) lds_st<float>(sm, L_P + (wave * 16 + lane) * 4, acc.x);
    }
    wg_barrier();
    if (wave == 0) {
      const int col = lane & 15;
      float v = 0.f;
#pragma unroll
      for (int w = 0; w < 8; ++w) v += lds_ld<float>(sm, L_P + (w * 16 + col) * 4);
      const float o = lds_ld<float>(sm, L_D16 + 192 + col * 4) + rbf(lds_ld<float>(sm, L_D16 + 128 + col * 4) * rbf(v));
      const u32 pr = pack_bf(o, dpp<XOR1>(o));
      if (lane < 16 && !(lane & 1)) ((GAS u32*)out)[dj * 8 + (lane >> 1)] = pr;
      if (dj == 0 && lane == 0) {
        const u64 e = gld(ws, WS_EPOCH);
        gst(ws, WS_EPOCH, e + 1);
      }
    }
    STAMP(11);
  }
  STAMP_FLUSH();
}

extern "C" void kernel_launch(void* const* d_in, const int* in_sizes, int n_in, void* d_out, int out_size, void* d_ws, size_t ws_size, hipStream_t stream) {
  fused_decode_kernel<<<256, 512, 0, stream>>>((const u16*)d_in[0], (const u16*)d_in[1], (const u16*)d_in[2], (const u16*)d_in[3], (const u16*)d_in[4],
                                               (const u16*)d_in[6], (const u16*)d_in[7], (const u16*)d_in[8], (const u16*)d_in[9], (const u16*)d_in[10],
                                               (u16*)d_out, (unsigned char*)d_ws);
}
```

```cpp
#include <hip/hip_runtime.h>
#include <hip/hip_bf16.h>
#include <stdint.h>

typedef unsigned short u16;
typedef unsigned int u32;
typedef unsigned long long u64;
typedef u32 u32x4 __attribute__((ext_vector_type(4)));
typedef u32 u32x2 __attribute__((ext_vector_type(2)));
typedef float f32x4 __attribute__((ext_vector_type(4)));
typedef float f32x2 __attribute__((ext_vector_type(2)));
typedef __bf16 bf16x8 __attribute__((ext_vector_type(8)));
typedef __bf16 bf16x2 __attribute__((ext_vector_type(2)));
typedef short s16x4 __attribute__((ext_vector_type(4)));
typedef short s16x8 __attribute__((ext_vector_type(8)));
#define GAS __attribute__((address_space(1)))
#define LAS __attribute__((address_space(3)))
#define RLX_AGENT __ATOMIC_RELAXED, __HIP_MEMORY_SCOPE_AGENT

constexpr int DM = 640, NH = 32, DH = 128, SK = 16384, FF = 1728;
constexpr u32 SPIN_CAP = 1u << 16;

constexpr int RB = 8, RC = 4;
constexpr u32 WS_EPOCH = 0;
constexpr u32 WS_MOD = 4096;
constexpr u32 WS_Q = WS_MOD + 8 * 3840 * 8;
constexpr u32 WS_A0 = WS_Q + 32 * 128 * 8;
constexpr u32 WS_A = WS_A0 + 256 * 144 * 16;
constexpr u32 WS_B = WS_A + 40 * 32 * 16 * 16;
constexpr u32 WS_C = WS_B + RB * 640 * 8;
constexpr u32 WS_END = WS_C + RC * 1792 * 8;

constexpr u32 L_WMOD = 0, L_WQ = 20480, L_WO = 40960, L_WF = 61440;
constexpr u32 L_SC = L_WF + 56 * 1024;
constexpr u32 L_H = L_SC + 1280;
constexpr u32 L_CB1 = L_H + 1280;
constexpr u32 L_SB = L_CB1 + 1280;
constexpr u32 L_Q = L_SB + 1280;
constexpr u32 L_RED = L_Q + 256;
constexpr u32 L_ACC = L_RED + 512;
constexpr u32 L_ML = L_ACC + 4096;
constexpr u32 L_O = L_ML + 64;
constexpr u32 L_OML = L_O + 4096;
constexpr u32 L_OB = L_OML + 64;
constexpr u32 L_X2 = L_OB + 4096;
constexpr u32 L_P = L_X2 + 1280;
constexpr u32 L_D16 = L_P + 512;
constexpr u32 L_FLAG = L_D16 + 256;
constexpr u32 LDS_BYTES = L_FLAG + 64;

__device__ __forceinline__ float bf_lo(u32 p) { return __uint_as_float(p << 16); }
__device__ __forceinline__ float bf_hi(u32 p) { return __uint_as_float(p & 0xffff0000u); }
__device__ __forceinline__ u32 pack_bf(float lo, float hi) {
  f32x2 v = {lo, hi};
  return __builtin_bit_cast(u32, __builtin_convertvector(v, bf16x2));
}
__device__ __forceinline__ float rbf(float f) { return bf_lo(pack_bf(f, f)); }
__device__ __forceinline__ float siluf(float x) { return x / (1.f + __expf(-x)); }
template <int CTRL> __device__ __forceinline__ float dpp(float x) {
  return __builtin_bit_cast(float, __builtin_amdgcn_mov_dpp(__builtin_bit_cast(int, x), CTRL, 0xf, 0xf, true));
}
template <int CTRL> __device__ __forceinline__ u32 dppu(u32 x) { return (u32)__builtin_amdgcn_mov_dpp((int)x, CTRL, 0xf, 0xf, true); }
constexpr int XOR1 = 0xB1, XOR2 = 0x4E, HMIR = 0x141, MIR = 0x140, ROR8 = 0x128, BCAST = 0x150;
__device__ __forceinline__ float xrow_sum(float x) {
  auto s = __builtin_amdgcn_permlane16_swap(__float_as_uint(x), __float_as_uint(x), false, false);
  x = __uint_as_float(s[0]) + __uint_as_float(s[1]);
  auto t = __builtin_amdgcn_permlane32_swap(__float_as_uint(x), __float_as_uint(x), false, false);
  return __uint_as_float(t[0]) + __uint_as_float(t[1]);
}
__device__ __forceinline__ float xrow_max(float x) {
  auto s = __builtin_amdgcn_permlane16_swap(__float_as_uint(x), __float_as_uint(x), false, false);
  x = fmaxf(__uint_as_float(s[0]), __uint_as_float(s[1]));
  auto t = __builtin_amdgcn_permlane32_swap(__float_as_uint(x), __float_as_uint(x), false, false);
  return fmaxf(__uint_as_float(t[0]), __uint_as_float(t[1]));
}
__device__ __forceinline__ float wave_sum(float x) {
  x += dpp<XOR1>(x); x += dpp<XOR2>(x); x += dpp<HMIR>(x); x += dpp<MIR>(x);
  return xrow_sum(x);
}
__device__ __forceinline__ float wave_max(float x) {
  x = fmaxf(x, dpp<XOR1>(x)); x = fmaxf(x, dpp<XOR2>(x)); x = fmaxf(x, dpp<HMIR>(x)); x = fmaxf(x, dpp<MIR>(x));
  return xrow_max(x);
}
__device__ __forceinline__ void lgkm0() { asm volatile("s_waitcnt lgkmcnt(0)" ::: "memory"); }
__device__ __forceinline__ void vm0() { asm volatile("s_waitcnt vmcnt(0)" ::: "memory"); }
__device__ __forceinline__ void wg_barrier() { lgkm0(); __builtin_amdgcn_s_barrier(); asm volatile("" ::: "memory"); }
__device__ __forceinline__ u64 gld(const unsigned char* ws, u32 off) { return __hip_atomic_load((GAS u64*)(ws + off), RLX_AGENT); }
__device__ __forceinline__ void gst(unsigned char* ws, u32 off, u64 v) { __hip_atomic_store((GAS u64*)(ws + off), v, RLX_AGENT); }
__device__ __forceinline__ bool all_ok(bool ok) { return __builtin_amdgcn_ballot_w64(!ok) == 0; }

template <typename T> __device__ __forceinline__ T lds_ld(LAS unsigned char* s, u32 off) { return *(LAS T*)(s + off); }
template <typename T> __device__ __forceinline__ void lds_st(LAS unsigned char* s, u32 off, T v) { *(LAS T*)(s + off) = v; }
__device__ __forceinline__ void lds_wait(LAS unsigned char* s, u32 off, u32 target) {
  for (u32 n = 0; n < (SPIN_CAP << 4); ++n) {
    const u32 v = (u32)__builtin_amdgcn_readfirstlane((int)*(volatile LAS u32*)(s + off));
    if (v >= target) break;
    __builtin_amdgcn_s_sleep(1);
  }
  asm volatile("" ::: "memory");
}
__device__ __forceinline__ void lds_arrive(LAS unsigned char* s, u32 off, int lane) {
  lgkm0();
  if (lane == 0) __hip_atomic_fetch_add((LAS u32*)(s + off), 1u, __ATOMIC_RELAXED, __HIP_MEMORY_SCOPE_WORKGROUP);
}
__device__ __forceinline__ void dma16(u32 voff, u64 base, u32 lds) {
  u32 keep;
  asm volatile("s_mov_b32 %0, m0\n\ts_mov_b32 m0, %3\n\ts_nop 0\n\tglobal_load_lds_dwordx4 %1, %2 nt\n\ts_mov_b32 m0, %0"
               : "=&s"(keep) : "v"(voff), "s"(base), "s"(lds) : "memory");
}
__device__ __forceinline__ void dma16g(const u16* g, u32 lds) {
  u32 keep;
  asm volatile("s_mov_b32 %0, m0\n\ts_mov_b32 m0, %2\n\ts_nop 0\n\tglobal_load_lds_dwordx4 %1, off nt\n\ts_mov_b32 m0, %0"
               : "=&s"(keep) : "v"(g), "s"(lds) : "memory");
}
__device__ __forceinline__ void touch(u32 voff, u64 base, u32 lds) {
  u32 keep;
  asm volatile("s_mov_b32 %0, m0\n\ts_mov_b32 m0, %3\n\ts_nop 0\n\tglobal_load_lds_dword %1, %2\n\ts_mov_b32 m0, %0"
               : "=&s"(keep) : "v"(voff), "s"(base), "s"(lds) : "memory");
}
template <int N> __device__ __forceinline__ void vmn() { asm volatile("s_waitcnt vmcnt(%0)" :: "i"(N) : "memory"); }
__device__ __forceinline__ void gld2_issue(u32x4& r, u32 voff, u64 base) {
  asm volatile("global_load_dwordx4 %0, %1, %2 sc1" : "=&v"(r) : "v"(voff), "s"(base) : "memory");
}
struct Tag { u32 lo, hi; };
__device__ __forceinline__ bool tag_ok(const u32x4& g, const Tag& t) {
  return (((g.x ^ t.lo) >> 16) | (g.y ^ t.hi) | ((g.z ^ t.lo) >> 16) | (g.w ^ t.hi)) == 0;
}
__device__ __forceinline__ u32 payload(const u32x4& g) { return __builtin_amdgcn_perm(g.z, g.x, 0x05040100u); }
__device__ __forceinline__ void put16(unsigned char* ws, u32 off, u32 v16, const Tag& t) { gst(ws, off, ((u64)t.hi << 32) | t.lo | v16); }
__device__ __forceinline__ void put32(u64 base, u32 voff, u32 v, const Tag& t) {
  const u32x4 g = {(v & 0xffffu) | t.lo, t.hi, (v >> 16) | t.lo, t.hi};
  asm volatile("global_store_dwordx4 %0, %1, %2 sc1\n\ts_nop 1" :: "v"(voff), "v"(g), "s"(base) : "memory");
}
template <int N> __device__ __forceinline__ void poll(u32x4 (&g)[N], const u32 (&voff)[N], u64 base, const Tag& t, int nap = 0) {
  for (u32 n = 0; n < SPIN_CAP; ++n) {
#pragma unroll
    for (int i = 0; i < N; ++i) gld2_issue(g[i], voff[i], base);
    asm volatile("s_waitcnt vmcnt(0)" ::: "memory");
    bool ok = true;
#pragma unroll
    for (int i = 0; i < N; ++i) { asm volatile("" : "+v"(g[i])); ok = ok && tag_ok(g[i], t); }
    if (all_ok(ok)) break;
    if (nap) __builtin_amdgcn_s_sleep(8);
  }
}
__device__ __forceinline__ bf16x8 tr_frag(LAS unsigned char* s, u32 off, int lane) {
  const u32 a = off + (u32)((8 * (lane >> 4) + ((lane >> 2) & 3)) * 32 + (lane & 3) * 8);
  const s16x4 r0 = __builtin_amdgcn_ds_read_tr16_b64_v4i16((LAS s16x4*)(s + a));
  const s16x4 r1 = __builtin_amdgcn_ds_read_tr16_b64_v4i16((LAS s16x4*)(s + a + 128));
  const s16x8 r = __builtin_shufflevector(r0, r1, 0, 1, 2, 3, 4, 5, 6, 7);
  return __builtin_bit_cast(bf16x8, r);
}
__device__ __forceinline__ bf16x8 a_frag(LAS unsigned char* s, u32 off, int lane) {
  return __builtin_bit_cast(bf16x8, lds_ld<u32x4>(s, off + (u32)(lane >> 4) * 16));
}
__device__ __forceinline__ f32x4 mfma(bf16x8 a, bf16x8 b, f32x4 c) { return __builtin_amdgcn_mfma_f32_16x16x32_bf16(a, b, c, 0, 0, 0); }

struct Tile { u32x4 k[4], v[4]; };
template <int OFF> __device__ __forceinline__ void ldnt(u32x4& r, u32 voff, u64 base) {
  asm volatile("global_load_dwordx4 %0, %1, %2 offset:%3 nt" : "+v"(r) : "v"(voff), "s"(base), "i"(OFF));
}
__device__ __forceinline__ void issue(Tile& t, u32 voff, u64 kb, u64 vb) {
  ldnt<0>(t.k[0], voff, kb); ldnt<1024>(t.k[1], voff, kb); ldnt<2048>(t.k[2], voff, kb); ldnt<3072>(t.k[3], voff, kb);
  ldnt<0>(t.v[0], voff, vb); ldnt<1024>(t.v[1], voff, vb); ldnt<2048>(t.v[2], voff, vb); ldnt<3072>(t.v[3], voff, vb);
}
__device__ __forceinline__ void pin(Tile& t) {
  asm volatile("" : "+v"(t.k[0]), "+v"(t.k[1]), "+v"(t.k[2]), "+v"(t.k[3]), "+v"(t.v[0]), "+v"(t.v[1]), "+v"(t.v[2]), "+v"(t.v[3]));
}
struct Attn { float m, l; f32x4 acc; u32x4 q; u32 sel[4]; bool lo8, b4; };
__device__ __forceinline__ void wait_all(Attn& a) {
  asm volatile("s_waitcnt vmcnt(0)" : "+v"(a.acc), "+v"(a.l));
}
__device__ __forceinline__ float dot2(u32 a, u32 b, float c) {
  return __builtin_amdgcn_fdot2_f32_bf16(__builtin_bit_cast(bf16x2, a), __builtin_bit_cast(bf16x2, b), c, false);
}
__device__ __forceinline__ void consume(const Tile& t, Attn& a) {
  float s[4];
#pragma unroll
  for (int u = 0; u < 4; ++u) s[u] = dot2(t.k[u].w, a.q.w, dot2(t.k[u].z, a.q.z, dot2(t.k[u].y, a.q.y, dot2(t.k[u].x, a.q.x, 0.f))));
  const float a0 = s[0] + dpp<ROR8>(s[0]), a1 = s[1] + dpp<ROR8>(s[1]), a2 = s[2] + dpp<ROR8>(s[2]), a3 = s[3] + dpp<ROR8>(s[3]);
  const float t01 = a.lo8 ? a0 : a1, t23 = a.lo8 ? a2 : a3;
  const float wa = t01 + dpp<HMIR>(t01), wb = t23 + dpp<HMIR>(t23);
  float w = a.b4 ? wb : wa;
  w += dpp<XOR1>(w); w += dpp<XOR2>(w);
  const float sc = rbf(rbf(w) * 0.08838834764831845f);
  if (__builtin_amdgcn_ballot_w64(sc > a.m) != 0) {
    const float mx = fmaxf(a.m, wave_max(sc));
    const float f = __builtin_amdgcn_exp2f((a.m - mx) * 1.4426950408889634f);
    a.acc *= f; a.l *= f; a.m = mx;
  }
  const float p = __builtin_amdgcn_exp2f((sc - a.m) * 1.4426950408889634f);
  a.l += p;
  const float lo = p - __uint_as_float(__float_as_uint(p) & 0xffff0000u);
  const u32 pk = __builtin_amdgcn_perm(__float_as_uint(p), __float_as_uint(lo), 0x07060302u);
  const u32 pu[4] = {dppu<BCAST + 0>(pk), dppu<BCAST + 8>(pk), dppu<BCAST + 4>(pk), dppu<BCAST + 12>(pk)};
#pragma unroll
  for (int u = 0; u < 4; ++u) {
    u32x4 av;
    av.x = __builtin_amdgcn_perm(0u, pu[u], a.sel[0]); av.y = __builtin_amdgcn_perm(0u, pu[u], a.sel[1]);
    av.z = __builtin_amdgcn_perm(0u, pu[u], a.sel[2]); av.w = __builtin_amdgcn_perm(0u, pu[u], a.sel[3]);
    a.acc = mfma(__builtin_bit_cast(bf16x8, av), __builtin_bit_cast(bf16x8, t.v[u]), a.acc);
  }
}

struct LN { float meanb, den; };
__device__ __forceinline__ LN ln_stats(const u32 (&xp)[5]) {
  float s = 0.f;
#pragma unroll
  for (int i = 0; i < 5; ++i) s += bf_lo(xp[i]) + bf_hi(xp[i]);
  const float mean = wave_sum(s) * (1.f / 640.f);
  float v = 0.f;
#pragma unroll
  for (int i = 0; i < 5; ++i) { const float d0 = bf_lo(xp[i]) - mean, d1 = bf_hi(xp[i]) - mean; v += d0 * d0 + d1 * d1; }
  const float var = rbf(wave_sum(v) * (1.f / 640.f));
  LN r; r.meanb = rbf(mean); r.den = rbf(sqrtf(rbf(var + 1e-5f)));
  return r;
}
__device__ __forceinline__ float ln_mod(float x, const LN& n, float c1, float sh) {
  return rbf(rbf(rbf(rbf(x - n.meanb) / n.den) * c1) + sh);
}

#define STAMP(i) do {} while (0)
#define HSTAMP(i) do {} while (0)
#define TSTAMP(i) do {} while (0)
#define STAMP_FLUSH() do {} while (0)

__global__ void __launch_bounds__(512, 2) fused_decode_kernel(
    const u16* __restrict__ x, const u16* __restrict__ kc, const u16* __restrict__ vc, const u16* __restrict__ wq,
    const u16* __restrict__ wo, const u16* __restrict__ wg, const u16* __restrict__ wu, const u16* __restrict__ wd,
    const u16* __restrict__ cnd, const u16* __restrict__ wmod, u16* __restrict__ out, unsigned char* ws) {
  __shared__ __attribute__((aligned(16))) unsigned char smem_[LDS_BYTES];
  LAS unsigned char* sm = (LAS unsigned char*)smem_;
  const int tid = threadIdx.x, lane = tid & 63;
  const int wave = __builtin_amdgcn_readfirstlane(tid >> 6);
  const int xcd = blockIdx.x & 7, j = blockIdx.x >> 3;
  const int head = 4 * xcd + (j >> 3), split = j & 7;
  const bool is_d = j >= 27;
  const int dj = 5 * xcd + (j - 27), fj = 27 * xcd + j;
  STAMP(0);

  constexpr u32 F_C1 = L_FLAG, F_C2 = L_FLAG + 4, F_Q = L_FLAG + 8, F_LATE = L_FLAG + 12;
  const u32 lds0 = (u32)(size_t)sm;
  const u64 next = *(const __attribute__((address_space(4))) u64*)(ws + WS_EPOCH) + 1;
  const Tag tag = {(u32)(next << 16), (u32)(next >> 16)};
  const u64 wsb = (u64)ws;
  const bool is_mod = j < 30, crit = j < 10;
  const int r2 = lane >> 1, c8 = (lane & 1) * 8;

  if (__builtin_expect(wave < 4, 1)) {
    Tile t0, t1, t2, t3, t4, t5;
    const u32 voff = (u32)wave * 4096u + (u32)lane * 16u;
    const u64 kb = (u64)kc + ((u64)head * SK + (u64)split * 2048) * 256, vb = (u64)vc + ((u64)head * SK + (u64)split * 2048) * 256;
#define UNDEF(T) asm volatile("" : "=v"(T.k[0]), "=v"(T.k[1]), "=v"(T.k[2]), "=v"(T.k[3]), "=v"(T.v[0]), "=v"(T.v[1]), "=v"(T.v[2]), "=v"(T.v[3]))
    if (tid < 16) lds_st<u32>(sm, L_FLAG + tid * 4, 0u);
    UNDEF(t0);
    if (crit) wg_barrier();
    issue(t0, voff, kb, vb);
    if (!crit) wg_barrier();
    STAMP(5);
    Attn a;
    a.m = -INFINITY; a.l = 0.f; a.acc = f32x4{0.f, 0.f, 0.f, 0.f};
    {
      const int i = lane & 15;
      a.lo8 = i < 8; a.b4 = (i & 4) != 0;
      const u32 src = i < 8 ? 0x0302u : 0x0100u;
      const u32 one = (i & 1) ? ((src << 16) | 0x0c0cu) : (0x0c0c0000u | src);
#pragma unroll
      for (int r = 0; r < 4; ++r) a.sel[r] = (((i & 7) >> 1) == r) ? one : 0x0c0c0c0cu;
    }
    UNDEF(t1); UNDEF(t2); UNDEF(t3); UNDEF(t4); UNDEF(t5);
    constexpr u64 TS = 16384;
    wait_all(a); HSTAMP(12); issue(t1, voff, kb + 1 * TS, vb + 1 * TS); pin(t0);
    wait_all(a); HSTAMP(13); issue(t2, voff, kb + 2 * TS, vb + 2 * TS); pin(t1);
    wait_all(a); HSTAMP(14); issue(t3, voff, kb + 3 * TS, vb + 3 * TS); pin(t2);
    wait_all(a); HSTAMP(15); issue(t4, voff, kb + 4 * TS, vb + 4 * TS); pin(t3);
    wait_all(a); issue(t5, voff, kb + 5 * TS, vb + 5 * TS); pin(t4);
    STAMP(1);
    lds_wait(sm, F_Q, 1);
    STAMP(2);
    a.q = lds_ld<u32x4>(sm, L_Q + (lane & 15) * 16);
    consume(t0, a);
    wait_all(a); issue(t0, voff, kb + 6 * TS, vb + 6 * TS); pin(t5);
    consume(t1, a); consume(t2, a); consume(t3, a);
    wait_all(a); issue(t1, voff, kb + 7 * TS, vb + 7 * TS); pin(t0);
    consume(t4, a); consume(t5, a); consume(t0, a);
    STAMP(3);
    for (int t = 8; t < 32; t += 2) {
      wait_all(a); issue(t0, voff, kb + (u64)t * TS, vb + (u64)t * TS); pin(t1);
      if (t == 24 && tid == 0) *(volatile LAS u32*)(sm + F_LATE) = 1u;
      consume(t1, a);
      wait_all(a); issue(t1, voff, kb + (u64)(t + 1) * TS, vb + (u64)(t + 1) * TS); pin(t0);
      consume(t0, a);
    }
    wait_all(a); pin(t1);
    STAMP(4);
    consume(t1, a);
    const float lw = wave_sum(a.l) * 0.25f;
    lds_st<f32x4>(sm, L_ACC + wave * 1024 + lane * 16, a.acc);
    if (lane == 0) lds_st<f32x2>(sm, L_ML + wave * 8, f32x2{a.m, lw});
  } else {
    const int hw = wave - 4;
    const int ll = lane < 40 ? lane : 39;
    const int k0 = 160 * hw + 4 * ll;
    const int mblk = crit ? 10 * xcd + j : 80 + 20 * xcd + (j - 10);
    u32x2 cv; u32 xk[3], xp[5]; int kk[3];
#pragma unroll
    for (int i = 0; i < 3; ++i) kk[i] = 160 * hw + 64 * i + (i < 2 ? lane : (lane & 31));
    const u32 vm_ = (u32)(r2 * 3840 + c8) * 2, vq_ = (u32)(r2 * 4096 + c8) * 2;
    const u64 mb = (u64)wmod + ((u64)160 * hw * 3840 + mblk * 16) * 2;
    asm volatile("global_load_dwordx2 %0, %1, %2" : "=&v"(cv) : "v"((u32)k0 * 2), "s"((u64)cnd) : "memory");
    if (crit) {
#pragma unroll
      for (int i = 0; i < 5; ++i) dma16(vm_, mb + (u64)i * 32 * 3840 * 2, lds0 + L_WMOD + (hw * 5 + i) * 1024);
      touch(WS_MOD, wsb, lds0 + L_OB + hw * 256);
    }
    wg_barrier();
    const int chunk = mblk / 40, mcol = (mblk % 40) * 16 + (lane & 15);
    const u32 mslot = (u32)((chunk < 2 ? 0 : (chunk == 3 || chunk == 4) ? 1 : 2) * 1280 + 2 * mcol + ((chunk == 1 || chunk >= 4) ? 1 : 0));
    auto mod_gemv = [&]() {
      f32x4 acc = {0.f, 0.f, 0.f, 0.f};
#pragma unroll
      for (int i = 0; i < 5; ++i) acc = mfma(a_frag(sm, L_SC + (160 * hw + 32 * i) * 2, lane), tr_frag(sm, L_WMOD + (hw * 5 + i) * 1024, lane), acc);
      if (lane < 16) lds_st<float>(sm, L_RED + (hw * 16 + lane) * 4, acc.x);
      lds_arrive(sm, F_C1, lane);
      if (hw == 0) {
        lds_wait(sm, F_C1, 4);
        const int col = lane & 15;
        const float v = lds_ld<float>(sm, L_RED + col * 4) + lds_ld<float>(sm, L_RED + (16 + col) * 4) + lds_ld<float>(sm, L_RED + (32 + col) * 4) + lds_ld<float>(sm, L_RED + (48 + col) * 4);
        const u32 v16 = pack_bf(v, v) & 0xffffu;
        const u32 o = WS_MOD + (u32)(((lane >> 4) * 3840 + mslot) * 8);
        put16(ws, o, v16, tag); put16(ws, o + 4 * 3840 * 8, v16, tag);
      }
    };
    if (crit) {
      asm volatile("s_waitcnt vmcnt(6)" : "+v"(cv) :: "memory");
      lds_st<u32x2>(sm, L_SC + k0 * 2, u32x2{pack_bf(siluf(bf_lo(cv.x)), siluf(bf_hi(cv.x))), pack_bf(siluf(bf_lo(cv.y)), siluf(bf_hi(cv.y)))});
      vmn<1>(); lgkm0();
      HSTAMP(13);
      mod_gemv();
      HSTAMP(14);
    }
#pragma unroll
    for (int i = 0; i < 5; ++i) asm volatile("global_load_dword %0, %1, %2 offset:%3" : "=&v"(xp[i]) : "v"((u32)lane * 4), "s"((u64)x), "i"(256 * i) : "memory");
#pragma unroll
    for (int i = 0; i < 3; ++i) asm volatile("global_load_ushort %0, %1, %2" : "=&v"(xk[i]) : "v"((u32)kk[i] * 2), "s"((u64)x) : "memory");
    {
      const u64 qb = (u64)wq + ((u64)160 * hw * 4096 + head * 128 + split * 16) * 2;
#pragma unroll
      for (int i = 0; i < 5; ++i) dma16(vq_, qb + (u64)i * 32 * 4096 * 2, lds0 + L_WQ + (hw * 5 + i) * 1024);
    }
    asm volatile("s_waitcnt vmcnt(5)" : "+v"(cv), "+v"(xp[0]), "+v"(xp[1]), "+v"(xp[2]), "+v"(xp[3]), "+v"(xp[4]), "+v"(xk[0]), "+v"(xk[1]), "+v"(xk[2]) :: "memory");
    const LN n1 = ln_stats(xp);
    if (is_mod && !crit) lds_st<u32x2>(sm, L_SC + k0 * 2, u32x2{pack_bf(siluf(bf_lo(cv.x)), siluf(bf_hi(cv.x))), pack_bf(siluf(bf_lo(cv.y)), siluf(bf_hi(cv.y)))});
    HSTAMP(12);
    vmn<0>();
    STAMP(1);
    bf16x8 bq[5];
#pragma unroll
    for (int i = 0; i < 5; ++i) bq[i] = tr_frag(sm, L_WQ + (hw * 5 + i) * 1024, lane);
    const u64 mrep = wsb + WS_MOD + (u64)xcd * 3840 * 8;
    {
      u32x4 g[3];
      const u32 vo[3] = {(u32)kk[0] * 16, (u32)kk[1] * 16, (u32)kk[2] * 16};
      poll<3>(g, vo, mrep, tag);
      STAMP(2);
#pragma unroll
      for (int i = 0; i < 3; ++i) {
        const float h = ln_mod(__uint_as_float(xk[i] << 16), n1, rbf(1.f + __uint_as_float(g[i].z << 16)), __uint_as_float(g[i].x << 16));
        lds_st<u16>(sm, L_H + kk[i] * 2, (u16)(__float_as_uint(h) >> 16));
      }
      lgkm0();
      f32x4 acc = {0.f, 0.f, 0.f, 0.f};
#pragma unroll
      for (int i = 0; i < 5; ++i) acc = mfma(a_frag(sm, L_H + (160 * hw + 32 * i) * 2, lane), bq[i], acc);
      if (lane < 16) lds_st<float>(sm, L_RED + 256 + (hw * 16 + lane) * 4, acc.x);
      lds_arrive(sm, F_C2, lane);
    }
    if (hw == 0) {
      lds_wait(sm, F_C2, 4);
      const int col = lane & 15;
      const float v = lds_ld<float>(sm, L_RED + 256 + col * 4) + lds_ld<float>(sm, L_RED + 256 + (16 + col) * 4) + lds_ld<float>(sm, L_RED + 256 + (32 + col) * 4) + lds_ld<float>(sm, L_RED + 256 + (48 + col) * 4);
      if (lane < 16) put16(ws, WS_Q + (u32)(head * 128 + split * 16 + lane) * 8, pack_bf(v, v) & 0xffffu, tag);
      STAMP(3);
      u32x4 g[1];
      const u32 vo[1] = {(u32)lane * 16};
      poll<1>(g, vo, wsb + WS_Q + (u64)head * 1024, tag);
      lds_st<u32>(sm, L_Q + lane * 4, payload(g[0]));
      lgkm0();
      if (lane == 0) *(volatile LAS u32*)(sm + F_Q) = 1u;
      STAMP(4);
    } else {
      lds_wait(sm, F_Q, 1);
    }
    if (is_mod && !crit) {
#pragma unroll
      for (int i = 0; i < 5; ++i) dma16(vm_, mb + (u64)i * 32 * 3840 * 2, lds0 + L_WMOD + (hw * 5 + i) * 1024);
      vmn<0>(); lgkm0();
      mod_gemv();
    }
    {
      const u32 vo = (u32)(r2 * 640 + c8) * 2;
      const u64 b = (u64)wo + ((u64)head * 128 * 640 + split * 80) * 2;
#pragma unroll
      for (int i = 0; i < 5; ++i) {
        const int id = 5 * hw + i;
        dma16(vo, b + (u64)((32 * (id & 3)) * 640 + (id >> 2) * 16) * 2, lds0 + L_WO + id * 1024);
      }
      vmn<0>();
      if (!is_d) {
#pragma unroll
        for (int i = 0; i < 5; ++i) dma16g((lane & 1 ? wu : wg) + fj * 8 + (size_t)(160 * hw + 32 * i + r2) * FF, lds0 + L_WF + (5 * hw + i) * 1024);
      } else {
        const u64 bd_ = (u64)wd + dj * 32;
#pragma unroll
        for (int i = 0; i < 7; ++i) { const int s_ = 14 * hw + i; dma16(vo, bd_ + (u64)s_ * 32 * 640 * 2, lds0 + L_WF + s_ * 1024); }
        vmn<0>();
#pragma unroll
        for (int i = 7; i < 14; ++i) { const int s_ = 14 * hw + i; if (s_ < 54) dma16(vo, bd_ + (u64)s_ * 32 * 640 * 2, lds0 + L_WF + s_ * 1024); }
      }
      vmn<0>();
    }
    STAMP(5);
    lds_wait(sm, F_LATE, 1);
    if (!is_d) {
      u32x4 g[3];
      const u32 vo[3] = {(u32)kk[0] * 16 + 10240, (u32)kk[1] * 16 + 10240, (u32)kk[2] * 16 + 10240};
      poll<3>(g, vo, mrep, tag, 1);
#pragma unroll
      for (int i = 0; i < 3; ++i) {
        lds_st<u16>(sm, L_SB + kk[i] * 2, (u16)g[i].x);
        lds_st<u16>(sm, L_CB1 + kk[i] * 2, (u16)pack_bf(1.f + __uint_as_float(g[i].z << 16), 0.f));
      }
    } else if (hw == 1) {
      const int col = lane & 15;
      u32x4 g[1];
      const u32 vo[1] = {(u32)(20480 + (dj * 16 + col) * 16)};
      const u32 xc = ((const GAS u16*)x)[dj * 16 + col];
      poll<1>(g, vo, mrep, tag, 1);
      if (lane < 16) {
        lds_st<float>(sm, L_D16 + 64 + lane * 4, __uint_as_float(g[0].x << 16));
        lds_st<float>(sm, L_D16 + 128 + lane * 4, __uint_as_float(g[0].z << 16));
        lds_st<float>(sm, L_D16 + lane * 4, __uint_as_float(xc << 16));
      }
    }
  }

  wg_barrier();
  STAMP(6);
  const u64 a0blk = wsb + WS_A0 + (u64)head * 8 * 144 * 16;
  if (wave < 3) {
    const f32x2 p0 = lds_ld<f32x2>(sm, L_ML), p1 = lds_ld<f32x2>(sm, L_ML + 8), p2 = lds_ld<f32x2>(sm, L_ML + 16), p3 = lds_ld<f32x2>(sm, L_ML + 24);
    const float M = fmaxf(fmaxf(p0.x, p1.x), fmaxf(p2.x, p3.x));
    const float e0 = __expf(p0.x - M), e1 = __expf(p1.x - M), e2 = __expf(p2.x - M), e3 = __expf(p3.x - M);
    if (wave < 2) {
      const int d = tid, c = d >> 3, i = d & 7;
      const u32 o = L_ACC + (u32)(((i >> 2) * 16 + c) * 4 + (i & 3)) * 4;
      const float v = e0 * (lds_ld<float>(sm, o) + lds_ld<float>(sm, o + 512)) + e1 * (lds_ld<float>(sm, o + 1024) + lds_ld<float>(sm, o + 1536))
                    + e2 * (lds_ld<float>(sm, o + 2048) + lds_ld<float>(sm, o + 2560)) + e3 * (lds_ld<float>(sm, o + 3072) + lds_ld<float>(sm, o + 3584));
      put32(a0blk, (u32)(split * 144 + d) * 16, __float_as_uint(v), tag);
    } else if (lane < 2) {
      const float L = e0 * p0.y + e1 * p1.y + e2 * p2.y + e3 * p3.y;
      put32(a0blk, (u32)(split * 144 + 128 + lane) * 16, __float_as_uint(lane ? L : M), tag);
    }
  }
  TSTAMP(12);
  bf16x8 bo[4];
  if (wave < 5) {
#pragma unroll
    for (int t = 0; t < 4; ++t) bo[t] = tr_frag(sm, L_WO + (wave * 4 + t) * 1024, lane);
  }
  {
    u32x4 g[3];
    const u32 vo[3] = {(u32)(wave * 144 + lane) * 16, (u32)(wave * 144 + 64 + lane) * 16, (u32)(wave * 144 + 128 + (lane & 1)) * 16};
    poll<3>(g, vo, a0blk, tag);
    lds_st<u32>(sm, L_O + (wave * 128 + lane) * 4, payload(g[0]));
    lds_st<u32>(sm, L_O + (wave * 128 + 64 + lane) * 4, payload(g[1]));
    if (lane < 2) lds_st<u32>(sm, L_OML + (wave * 2 + lane) * 4, payload(g[2]));
  }
  STAMP(7);
  wg_barrier();
  TSTAMP(13);
  const u32 stage = L_OB + (u32)wave * 512;
  if (wave < 5) {
    const f32x2 ml = lds_ld<f32x2>(sm, L_OML + (lane & 7) * 8);
    float M = ml.x;
    M = fmaxf(M, dpp<XOR1>(M)); M = fmaxf(M, dpp<XOR2>(M)); M = fmaxf(M, dpp<HMIR>(M));
    const float e = __expf(ml.x - M);
    float L = e * ml.y;
    L += dpp<XOR1>(L); L += dpp<XOR2>(L); L += dpp<HMIR>(L);
    const float wn = e / L;
    float o0 = 0.f, o1 = 0.f;
#pragma unroll
    for (int s = 0; s < 8; ++s) {
      const float ws_ = __uint_as_float((u32)__builtin_amdgcn_readlane((int)__float_as_uint(wn), s));
      const f32x2 v = lds_ld<f32x2>(sm, L_O + (s * 128 + 2 * lane) * 4);
      o0 += ws_ * v.x; o1 += ws_ * v.y;
    }
    lds_st<u32>(sm, stage + lane * 4, pack_bf(o0, o1));
    lgkm0();
    f32x4 acc = {0.f, 0.f, 0.f, 0.f};
#pragma unroll
    for (int t = 0; t < 4; ++t) acc = mfma(a_frag(sm, stage + t * 64, lane), bo[t], acc);
    if (lane < 16) put32(wsb + WS_A, (u32)(((split * 5 + wave) * 32 + head) * 16 + lane) * 16, __float_as_uint(acc.x), tag);
  }
  STAMP(8);
  if (!is_d) {
    if (wave < 5) {
      bf16x8 bf[4];
#pragma unroll
      for (int t = 0; t < 4; ++t) bf[t] = tr_frag(sm, L_WF + (wave * 4 + t) * 1024, lane);
      const u32 cb1 = lds_ld<u32>(sm, L_CB1 + (wave * 64 + lane) * 4), sb = lds_ld<u32>(sm, L_SB + (wave * 64 + lane) * 4);
      u32x4 g[1];
      const u32 vo[1] = {(u32)(wave * 64 + lane) * 16};
      poll<1>(g, vo, wsb + WS_B + (u64)xcd * 640 * 8, tag);
      const u32 own = payload(g[0]);
      lds_st<u32>(sm, L_X2 + (wave * 64 + lane) * 4, own);
      STAMP(9);
      wg_barrier();
      TSTAMP(14);
      u32 xp[5];
#pragma unroll
      for (int i = 0; i < 5; ++i) xp[i] = lds_ld<u32>(sm, L_X2 + (i * 64 + lane) * 4);
      const LN n2 = ln_stats(xp);
      lds_st<u32>(sm, stage + lane * 4, pack_bf(ln_mod(bf_lo(own), n2, bf_lo(cb1), bf_lo(sb)), ln_mod(bf_hi(own), n2, bf_hi(cb1), bf_hi(sb))));
      lgkm0();
      f32x4 acc = {0.f, 0.f, 0.f, 0.f};
#pragma unroll
      for (int t = 0; t < 4; ++t) acc = mfma(a_frag(sm, stage + t * 64, lane), bf[t], acc);
      if (lane < 16) lds_st<float>(sm, L_P + (wave * 16 + lane) * 4, acc.x);
    } else {
      wg_barrier();
    }
    wg_barrier();
    TSTAMP(15);
    if (wave == 0) {
      const int col = lane & 15;
      float v = 0.f;
#pragma unroll
      for (int w = 0; w < 5; ++w) v += lds_ld<float>(sm, L_P + (w * 16 + col) * 4);
      v = rbf(v);
      const float u = dpp<ROR8>(v);
      const float act = rbf(rbf(siluf(v)) * u);
      if (col < 8) put16(ws, WS_C + (u32)((lane >> 4) * 1792 + fj * 8 + col) * 8, __float_as_uint(act) >> 16, tag);
    }
    STAMP(10);
  } else {
    if (wave == 7) {
      u32x4 g[8];
      u32 vo[8];
#pragma unroll
      for (int i = 0; i < 8; ++i) vo[i] = (u32)(((dj * 32 + 4 * i + (lane >> 4)) * 16 + (lane & 15)) * 16);
      poll<8>(g, vo, wsb + WS_A, tag);
      float y = 0.f;
#pragma unroll
      for (int i = 0; i < 8; ++i) y += __uint_as_float(payload(g[i]));
      y = rbf(xrow_sum(y));
      const int col = lane & 15;
      const float x2 = rbf(lds_ld<float>(sm, L_D16 + col * 4) + rbf(lds_ld<float>(sm, L_D16 + 64 + col * 4) * y));
      const u32 o = WS_B + (u32)((lane >> 4) * 640 + dj * 16 + col) * 8;
      put16(ws, o, __float_as_uint(x2) >> 16, tag); put16(ws, o + 4 * 640 * 8, __float_as_uint(x2) >> 16, tag);
      if (lane < 16) lds_st<float>(sm, L_D16 + 192 + lane * 4, x2);
      STAMP(9);
    }
    const int nst = wave == 7 ? 5 : 7;
    bf16x8 bd[7];
#pragma unroll
    for (int t = 0; t < 7; ++t) bd[t] = tr_frag(sm, L_WF + (wave * 7 + (t < nst ? t : 0)) * 1024, lane);
    wg_barrier();
    {
      const int ng = nst * 16;
      const int i1 = lane + 64 < ng ? lane + 64 : lane;
      u32x4 g[2];
      const u32 vo[2] = {(u32)(wave * 112 + lane) * 16, (u32)(wave * 112 + i1) * 16};
      poll<2>(g, vo, wsb + WS_C + (u64)(dj & 3) * 1792 * 8, tag);
      STAMP(10);
      lds_st<u32>(sm, stage + lane * 4, payload(g[0]));
      lds_st<u32>(sm, stage + i1 * 4, payload(g[1]));
      lgkm0();
      f32x4 acc = {0.f, 0.f, 0.f, 0.f};
#pragma unroll
      for (int t = 0; t < 7; ++t) if (t < nst) acc = mfma(a_frag(sm, stage + t * 64, lane), bd[t], acc);
      if (lane < 16) lds_st<float>(sm, L_P + (wave * 16 + lane) * 4, acc.x);
    }
    wg_barrier();
    if (wave == 0) {
      const int col = lane & 15;
      float v = 0.f;
#pragma unroll
      for (int w = 0; w < 8; ++w) v += lds_ld<float>(sm, L_P + (w * 16 + col) * 4);
      const float o = lds_ld<float>(sm, L_D16 + 192 + col * 4) + rbf(lds_ld<float>(sm, L_D16 + 128 + col * 4) * rbf(v));
      const u32 pr = pack_bf(o, dpp<XOR1>(o));
      if (lane < 16 && !(lane & 1)) ((GAS u32*)out)[dj * 8 + (lane >> 1)] = pr;
      if (dj == 0 && lane == 0) gst(ws, WS_EPOCH, next);
    }
    STAMP(11);
  }
  STAMP_FLUSH();
}

extern "C" void kernel_launch(void* const* d_in, const int* in_sizes, int n_in, void* d_out, int out_size, void* d_ws, size_t ws_size, hipStream_t stream) {
  fused_decode_kernel<<<256, 512, 0, stream>>>((const u16*)d_in[0], (const u16*)d_in[1], (const u16*)d_in[2], (const u16*)d_in[3], (const u16*)d_in[4],
                                               (const u16*)d_in[6], (const u16*)d_in[7], (const u16*)d_in[8], (const u16*)d_in[9], (const u16*)d_in[10],
                                               (u16*)d_out, (unsigned char*)d_ws);
}
```

```cpp
#define PROBE 0
#include <hip/hip_runtime.h>
#include <hip/hip_bf16.h>
#include <stdint.h>

typedef unsigned short u16;
typedef unsigned int u32;
typedef unsigned long long u64;
typedef u32 u32x4 __attribute__((ext_vector_type(4)));
typedef u32 u32x2 __attribute__((ext_vector_type(2)));
typedef float f32x4 __attribute__((ext_vector_type(4)));
typedef float f32x2 __attribute__((ext_vector_type(2)));
typedef __bf16 bf16x8 __attribute__((ext_vector_type(8)));
typedef __bf16 bf16x2 __attribute__((ext_vector_type(2)));
typedef short s16x4 __attribute__((ext_vector_type(4)));
typedef short s16x8 __attribute__((ext_vector_type(8)));
#define GAS __attribute__((address_space(1)))
#define LAS __attribute__((address_space(3)))
#define RLX_AGENT __ATOMIC_RELAXED, __HIP_MEMORY_SCOPE_AGENT

constexpr int DM = 640, NH = 32, DH = 128, SK = 16384, FF = 1728;
constexpr u32 SPIN_CAP = 1u << 16;

constexpr int RB = 8, RC = 4;
constexpr u32 WS_EPOCH = 0;
constexpr u32 WS_MOD = 4096;
constexpr u32 WS_Q = WS_MOD + 8 * 3840 * 8;
constexpr u32 WS_A0 = WS_Q + 32 * 128 * 8;
constexpr u32 WS_A = WS_A0 + 256 * 144 * 16;
constexpr u32 WS_B = WS_A + 40 * 32 * 16 * 16;
constexpr u32 WS_C = WS_B + RB * 640 * 8;
constexpr u32 WS_END = WS_C + RC * 1792 * 8;

constexpr u32 L_WMOD = 0, L_WQ = 20480, L_WO = 40960, L_WF = 61440;
constexpr u32 L_SC = L_WF + 56 * 1024;
constexpr u32 L_H = L_SC + 1280;
constexpr u32 L_CB1 = L_H + 1280;
constexpr u32 L_SB = L_CB1 + 1280;
constexpr u32 L_Q = L_SB + 1280;
constexpr u32 L_RED = L_Q + 256;
constexpr u32 L_ACC = L_RED + 512;
constexpr u32 L_ML = L_ACC + 4096;
constexpr u32 L_O = L_ML + 64;
constexpr u32 L_OML = L_O + 4096;
constexpr u32 L_OB = L_OML + 64;
constexpr u32 L_X2 = L_OB + 4096;
constexpr u32 L_P = L_X2 + 1280;
constexpr u32 L_D16 = L_P + 512;
constexpr u32 L_FLAG = L_D16 + 256;
constexpr u32 LDS_BYTES = L_FLAG + 64;

__device__ __forceinline__ float bf_lo(u32 p) { return __uint_as_float(p << 16); }
__device__ __forceinline__ float bf_hi(u32 p) { return __uint_as_float(p & 0xffff0000u); }
__device__ __forceinline__ u32 pack_bf(float lo, float hi) {
  f32x2 v = {lo, hi};
  return __builtin_bit_cast(u32, __builtin_convertvector(v, bf16x2));
}
__device__ __forceinline__ float rbf(float f) { return bf_lo(pack_bf(f, f)); }
__device__ __forceinline__ float siluf(float x) { return x / (1.f + __expf(-x)); }
template <int CTRL> __device__ __forceinline__ float dpp(float x) {
  return __builtin_bit_cast(float, __builtin_amdgcn_mov_dpp(__builtin_bit_cast(int, x), CTRL, 0xf, 0xf, true));
}
template <int CTRL> __device__ __forceinline__ u32 dppu(u32 x) { return (u32)__builtin_amdgcn_mov_dpp((int)x, CTRL, 0xf, 0xf, true); }
constexpr int XOR1 = 0xB1, XOR2 = 0x4E, HMIR = 0x141, MIR = 0x140, ROR8 = 0x128, BCAST = 0x150;
__device__ __forceinline__ float xrow_sum(float x) {
  auto s = __builtin_amdgcn_permlane16_swap(__float_as_uint(x), __float_as_uint(x), false, false);
  x = __uint_as_float(s[0]) + __uint_as_float(s[1]);
  auto t = __builtin_amdgcn_permlane32_swap(__float_as_uint(x), __float_as_uint(x), false, false);
  return __uint_as_float(t[0]) + __uint_as_float(t[1]);
}
__device__ __forceinline__ float xrow_max(float x) {
  auto s = __builtin_amdgcn_permlane16_swap(__float_as_uint(x), __float_as_uint(x), false, false);
  x = fmaxf(__uint_as_float(s[0]), __uint_as_float(s[1]));
  auto t = __builtin_amdgcn_permlane32_swap(__float_as_uint(x), __float_as_uint(x), false, false);
  return fmaxf(__uint_as_float(t[0]), __uint_as_float(t[1]));
}
__device__ __forceinline__ float wave_sum(float x) {
  x += dpp<XOR1>(x); x += dpp<XOR2>(x); x += dpp<HMIR>(x); x += dpp<MIR>(x);
  return xrow_sum(x);
}
__device__ __forceinline__ float wave_max(float x) {
  x = fmaxf(x, dpp<XOR1>(x)); x = fmaxf(x, dpp<XOR2>(x)); x = fmaxf(x, dpp<HMIR>(x)); x = fmaxf(x, dpp<MIR>(x));
  return xrow_max(x);
}
__device__ __forceinline__ void lgkm0() { asm volatile("s_waitcnt lgkmcnt(0)" ::: "memory"); }
__device__ __forceinline__ void vm0() { asm volatile("s_waitcnt vmcnt(0)" ::: "memory"); }
__device__ __forceinline__ void wg_barrier() { lgkm0(); __builtin_amdgcn_s_barrier(); asm volatile("" ::: "memory"); }
__device__ __forceinline__ u64 gld(const unsigned char* ws, u32 off) { return __hip_atomic_load((GAS u64*)(ws + off), RLX_AGENT); }
__device__ __forceinline__ void gst(unsigned char* ws, u32 off, u64 v) { __hip_atomic_store((GAS u64*)(ws + off), v, RLX_AGENT); }
__device__ __forceinline__ bool all_ok(bool ok) { return __builtin_amdgcn_ballot_w64(!ok) == 0; }

template <typename T> __device__ __forceinline__ T lds_ld(LAS unsigned char* s, u32 off) { return *(LAS T*)(s + off); }
template <typename T> __device__ __forceinline__ void lds_st(LAS unsigned char* s, u32 off, T v) { *(LAS T*)(s + off) = v; }
__device__ __forceinline__ void lds_wait(LAS unsigned char* s, u32 off, u32 target) {
  for (u32 n = 0; n < (SPIN_CAP << 4); ++n) {
    const u32 v = (u32)__builtin_amdgcn_readfirstlane((int)*(volatile LAS u32*)(s + off));
    if (v >= target) break;
    __builtin_amdgcn_s_sleep(1);
  }
  asm volatile("" ::: "memory");
}
__device__ __forceinline__ void lds_arrive(LAS unsigned char* s, u32 off, int lane) {
  lgkm0();
  if (lane == 0) __hip_atomic_fetch_add((LAS u32*)(s + off), 1u, __ATOMIC_RELAXED, __HIP_MEMORY_SCOPE_WORKGROUP);
}
__device__ __forceinline__ void dma16(u32 voff, u64 base, u32 lds) {
  u32 keep;
  asm volatile("s_mov_b32 %0, m0\n\ts_mov_b32 m0, %3\n\ts_nop 0\n\tglobal_load_lds_dwordx4 %1, %2 nt\n\ts_mov_b32 m0, %0"
               : "=&s"(keep) : "v"(voff), "s"(base), "s"(lds) : "memory");
}
__device__ __forceinline__ void dma16g(const u16* g, u32 lds) {
  u32 keep;
  asm volatile("s_mov_b32 %0, m0\n\ts_mov_b32 m0, %2\n\ts_nop 0\n\tglobal_load_lds_dwordx4 %1, off nt\n\ts_mov_b32 m0, %0"
               : "=&s"(keep) : "v"(g), "s"(lds) : "memory");
}
__device__ __forceinline__ void touch(u32 voff, u64 base, u32 lds) {
  u32 keep;
  asm volatile("s_mov_b32 %0, m0\n\ts_mov_b32 m0, %3\n\ts_nop 0\n\tglobal_load_lds_dword %1, %2\n\ts_mov_b32 m0, %0"
               : "=&s"(keep) : "v"(voff), "s"(base), "s"(lds) : "memory");
}
template <int N> __device__ __forceinline__ void vmn() { asm volatile("s_waitcnt vmcnt(%0)" :: "i"(N) : "memory"); }
__device__ __forceinline__ void gld2_issue(u32x4& r, u32 voff, u64 base) {
  asm volatile("global_load_dwordx4 %0, %1, %2 sc1" : "=&v"(r) : "v"(voff), "s"(base) : "memory");
}
struct Tag { u32 lo, hi; };
__device__ __forceinline__ bool tag_ok(const u32x4& g, const Tag& t) {
  return (((g.x ^ t.lo) >> 16) | (g.y ^ t.hi) | ((g.z ^ t.lo) >> 16) | (g.w ^ t.hi)) == 0;
}
__device__ __forceinline__ u32 payload(const u32x4& g) { return __builtin_amdgcn_perm(g.z, g.x, 0x05040100u); }
__device__ __forceinline__ void put16(unsigned char* ws, u32 off, u32 v16, const Tag& t) { gst(ws, off, ((u64)t.hi << 32) | t.lo | v16); }
__device__ __forceinline__ void put32(u64 base, u32 voff, u32 v, const Tag& t) {
  const u32x4 g = {(v & 0xffffu) | t.lo, t.hi, (v >> 16) | t.lo, t.hi};
  asm volatile("global_store_dwordx4 %0, %1, %2 sc1\n\ts_nop 1" :: "v"(voff), "v"(g), "s"(base) : "memory");
}
template <int N> __device__ __forceinline__ void poll(u32x4 (&g)[N], const u32 (&voff)[N], u64 base, const Tag& t, int nap = 0) {
  for (u32 n = 0; n < SPIN_CAP; ++n) {
#pragma unroll
    for (int i = 0; i < N; ++i) gld2_issue(g[i], voff[i], base);
    asm volatile("s_waitcnt vmcnt(0)" ::: "memory");
    bool ok = true;
#pragma unroll
    for (int i = 0; i < N; ++i) { asm volatile("" : "+v"(g[i])); ok = ok && tag_ok(g[i], t); }
    if (all_ok(ok)) break;
    if (nap) __builtin_amdgcn_s_sleep(8);
  }
}
__device__ __forceinline__ bf16x8 tr_frag(LAS unsigned char* s, u32 off, int lane) {
  const u32 a = off + (u32)((8 * (lane >> 4) + ((lane >> 2) & 3)) * 32 + (lane & 3) * 8);
  const s16x4 r0 = __builtin_amdgcn_ds_read_tr16_b64_v4i16((LAS s16x4*)(s + a));
  const s16x4 r1 = __builtin_amdgcn_ds_read_tr16_b64_v4i16((LAS s16x4*)(s + a + 128));
  const s16x8 r = __builtin_shufflevector(r0, r1, 0, 1, 2, 3, 4, 5, 6, 7);
  return __builtin_bit_cast(bf16x8, r);
}
__device__ __forceinline__ bf16x8 a_frag(LAS unsigned char* s, u32 off, int lane) {
  return __builtin_bit_cast(bf16x8, lds_ld<u32x4>(s, off + (u32)(lane >> 4) * 16));
}
__device__ __forceinline__ f32x4 mfma(bf16x8 a, bf16x8 b, f32x4 c) { return __builtin_amdgcn_mfma_f32_16x16x32_bf16(a, b, c, 0, 0, 0); }

struct Tile { u32x4 k[4], v[4]; };
template <int OFF> __device__ __forceinline__ void ldnt(u32x4& r, u32 voff, u64 base) {
  asm volatile("global_load_dwordx4 %0, %1, %2 offset:%3 nt" : "+v"(r) : "v"(voff), "s"(base), "i"(OFF));
}
__device__ __forceinline__ void issue(Tile& t, u32 voff, u64 kb, u64 vb) {
  ldnt<0>(t.k[0], voff, kb); ldnt<1024>(t.k[1], voff, kb); ldnt<2048>(t.k[2], voff, kb); ldnt<3072>(t.k[3], voff, kb);
  ldnt<0>(t.v[0], voff, vb); ldnt<1024>(t.v[1], voff, vb); ldnt<2048>(t.v[2], voff, vb); ldnt<3072>(t.v[3], voff, vb);
}
__device__ __forceinline__ void pin(Tile& t) {
  asm volatile("" : "+v"(t.k[0]), "+v"(t.k[1]), "+v"(t.k[2]), "+v"(t.k[3]), "+v"(t.v[0]), "+v"(t.v[1]), "+v"(t.v[2]), "+v"(t.v[3]));
}
struct Attn { float m, l; f32x4 acc; u32x4 q; u32 sel[4]; bool lo8, b4; };
__device__ __forceinline__ void wait_all(Attn& a) {
  asm volatile("s_waitcnt vmcnt(0)" : "+v"(a.acc), "+v"(a.l));
}
__device__ __forceinline__ float dot2(u32 a, u32 b, float c) {
  return __builtin_amdgcn_fdot2_f32_bf16(__builtin_bit_cast(bf16x2, a), __builtin_bit_cast(bf16x2, b), c, false);
}
__device__ __forceinline__ void consume(const Tile& t, Attn& a) {
  float s[4];
#pragma unroll
  for (int u = 0; u < 4; ++u) s[u] = dot2(t.k[u].w, a.q.w, dot2(t.k[u].z, a.q.z, dot2(t.k[u].y, a.q.y, dot2(t.k[u].x, a.q.x, 0.f))));
  const float a0 = s[0] + dpp<ROR8>(s[0]), a1 = s[1] + dpp<ROR8>(s[1]), a2 = s[2] + dpp<ROR8>(s[2]), a3 = s[3] + dpp<ROR8>(s[3]);
  const float t01 = a.lo8 ? a0 : a1, t23 = a.lo8 ? a2 : a3;
  const float wa = t01 + dpp<HMIR>(t01), wb = t23 + dpp<HMIR>(t23);
  float w = a.b4 ? wb : wa;
  w += dpp<XOR1>(w); w += dpp<XOR2>(w);
  const float sc = rbf(rbf(w) * 0.08838834764831845f);
  if (__builtin_amdgcn_ballot_w64(sc > a.m) != 0) {
    const float mx = fmaxf(a.m, wave_max(sc));
    const float f = __builtin_amdgcn_exp2f((a.m - mx) * 1.4426950408889634f);
    a.acc *= f; a.l *= f; a.m = mx;
  }
  const float p = __builtin_amdgcn_exp2f((sc - a.m) * 1.4426950408889634f);
  a.l += p;
  const float lo = p - __uint_as_float(__float_as_uint(p) & 0xffff0000u);
  const u32 pk = __builtin_amdgcn_perm(__float_as_uint(p), __float_as_uint(lo), 0x07060302u);
  const u32 pu[4] = {dppu<BCAST + 0>(pk), dppu<BCAST + 8>(pk), dppu<BCAST + 4>(pk), dppu<BCAST + 12>(pk)};
#pragma unroll
  for (int u = 0; u < 4; ++u) {
    u32x4 av;
    av.x = __builtin_amdgcn_perm(0u, pu[u], a.sel[0]); av.y = __builtin_amdgcn_perm(0u, pu[u], a.sel[1]);
    av.z = __builtin_amdgcn_perm(0u, pu[u], a.sel[2]); av.w = __builtin_amdgcn_perm(0u, pu[u], a.sel[3]);
    a.acc = mfma(__builtin_bit_cast(bf16x8, av), __builtin_bit_cast(bf16x8, t.v[u]), a.acc);
  }
}

struct LN { float meanb, den; };
__device__ __forceinline__ LN ln_stats(const u32 (&xp)[5]) {
  float s = 0.f;
#pragma unroll
  for (int i = 0; i < 5; ++i) s += bf_lo(xp[i]) + bf_hi(xp[i]);
  const float mean = wave_sum(s) * (1.f / 640.f);
  float v = 0.f;
#pragma unroll
  for (int i = 0; i < 5; ++i) { const float d0 = bf_lo(xp[i]) - mean, d1 = bf_hi(xp[i]) - mean; v += d0 * d0 + d1 * d1; }
  const float var = rbf(wave_sum(v) * (1.f / 640.f));
  LN r; r.meanb = rbf(mean); r.den = rbf(sqrtf(rbf(var + 1e-5f)));
  return r;
}
__device__ __forceinline__ float ln_mod(float x, const LN& n, float c1, float sh) {
  return rbf(rbf(rbf(rbf(x - n.meanb) / n.den) * c1) + sh);
}

#define STAMP(i) do {} while (0)
#define HSTAMP(i) do {} while (0)
#define TSTAMP(i) do {} while (0)
#define STAMP_FLUSH() do {} while (0)

__global__ void __launch_bounds__(512, 2) fused_decode_kernel(
    const u16* __restrict__ x, const u16* __restrict__ kc, const u16* __restrict__ vc, const u16* __restrict__ wq,
    const u16* __restrict__ wo, const u16* __restrict__ wg, const u16* __restrict__ wu, const u16* __restrict__ wd,
    const u16* __restrict__ cnd, const u16* __restrict__ wmod, u16* __restrict__ out, unsigned char* ws) {
  __shared__ __attribute__((aligned(16))) unsigned char smem_[LDS_BYTES];
  LAS unsigned char* sm = (LAS unsigned char*)smem_;
  const int tid = threadIdx.x, lane = tid & 63;
  const int wave = __builtin_amdgcn_readfirstlane(tid >> 6);
  const int xcd = blockIdx.x & 7, j = blockIdx.x >> 3;
  const int head = 4 * xcd + (j >> 3), split = j & 7;
  const bool is_d = j >= 27;
  const int dj = 5 * xcd + (j - 27), fj = 27 * xcd + j;
  STAMP(0);
  u64 pt0 = __builtin_amdgcn_s_memrealtime(), pt1 = pt0, pt2 = pt0, pt3 = pt0;

  constexpr u32 F_C1 = L_FLAG, F_C2 = L_FLAG + 4, F_Q = L_FLAG + 8, F_LATE = L_FLAG + 12;
  const u32 lds0 = (u32)(size_t)sm;
  const u64 next = *(const __attribute__((address_space(4))) u64*)(ws + WS_EPOCH) + 1;
  const Tag tag = {(u32)(next << 16), (u32)(next >> 16)};
  const u64 wsb = (u64)ws;
  const bool is_mod = j < 30, crit = j < 10;
  const int r2 = lane >> 1, c8 = (lane & 1) * 8;

  if (__builtin_expect(wave < 4, 1)) {
    Tile t0, t1, t2, t3, t4, t5;
    const u32 voff = (u32)wave * 4096u + (u32)lane * 16u;
    const u64 kb = (u64)kc + ((u64)head * SK + (u64)split * 2048) * 256, vb = (u64)vc + ((u64)head * SK + (u64)split * 2048) * 256;
#define UNDEF(T) asm volatile("" : "=v"(T.k[0]), "=v"(T.k[1]), "=v"(T.k[2]), "=v"(T.k[3]), "=v"(T.v[0]), "=v"(T.v[1]), "=v"(T.v[2]), "=v"(T.v[3]))
    if (tid < 16) lds_st<u32>(sm, L_FLAG + tid * 4, 0u);
    UNDEF(t0);
    if (crit) wg_barrier();
    issue(t0, voff, kb, vb);
    if (!crit) wg_barrier();
    STAMP(5);
    Attn a;
    a.m = -INFINITY; a.l = 0.f; a.acc = f32x4{0.f, 0.f, 0.f, 0.f};
    {
      const int i = lane & 15;
      a.lo8 = i < 8; a.b4 = (i & 4) != 0;
      const u32 src = i < 8 ? 0x0302u : 0x0100u;
      const u32 one = (i & 1) ? ((src << 16) | 0x0c0cu) : (0x0c0c0000u | src);
#pragma unroll
      for (int r = 0; r < 4; ++r) a.sel[r] = (((i & 7) >> 1) == r) ? one : 0x0c0c0c0cu;
    }
    UNDEF(t1); UNDEF(t2); UNDEF(t3); UNDEF(t4); UNDEF(t5);
    constexpr u64 TS = 16384;
    wait_all(a); pt1 = __builtin_amdgcn_s_memrealtime(); HSTAMP(12); issue(t1, voff, kb + 1 * TS, vb + 1 * TS); pin(t0);
    wait_all(a); HSTAMP(13); issue(t2, voff, kb + 2 * TS, vb + 2 * TS); pin(t1);
    wait_all(a); HSTAMP(14); issue(t3, voff, kb + 3 * TS, vb + 3 * TS); pin(t2);
    wait_all(a); HSTAMP(15); issue(t4, voff, kb + 4 * TS, vb + 4 * TS); pin(t3);
    wait_all(a); issue(t5, voff, kb + 5 * TS, vb + 5 * TS); pin(t4);
    STAMP(1);
    lds_wait(sm, F_Q, 1);
    STAMP(2);
    pt2 = __builtin_amdgcn_s_memrealtime();
    a.q = lds_ld<u32x4>(sm, L_Q + (lane & 15) * 16);
    consume(t0, a);
    wait_all(a); issue(t0, voff, kb + 6 * TS, vb + 6 * TS); pin(t5);
    consume(t1, a); consume(t2, a); consume(t3, a);
    wait_all(a); issue(t1, voff, kb + 7 * TS, vb + 7 * TS); pin(t0);
    consume(t4, a); consume(t5, a); consume(t0, a);
    STAMP(3);
    for (int t = 8; t < 32; t += 2) {
      wait_all(a); issue(t0, voff, kb + (u64)t * TS, vb + (u64)t * TS); pin(t1);
      if (t == 24 && tid == 0) *(volatile LAS u32*)(sm + F_LATE) = 1u;
      consume(t1, a);
      wait_all(a); issue(t1, voff, kb + (u64)(t + 1) * TS, vb + (u64)(t + 1) * TS); pin(t0);
      consume(t0, a);
    }
    wait_all(a); pin(t1);
    STAMP(4);
    pt3 = __builtin_amdgcn_s_memrealtime();
    consume(t1, a);
    const float lw = wave_sum(a.l) * 0.25f;
    lds_st<f32x4>(sm, L_ACC + wave * 1024 + lane * 16, a.acc);
    if (lane == 0) lds_st<f32x2>(sm, L_ML + wave * 8, f32x2{a.m, lw});
  } else {
    const int hw = wave - 4;
    const int ll = lane < 40 ? lane : 39;
    const int k0 = 160 * hw + 4 * ll;
    const int mblk = crit ? 10 * xcd + j : 80 + 20 * xcd + (j - 10);
    u32x2 cv; u32 xk[3], xp[5]; int kk[3];
#pragma unroll
    for (int i = 0; i < 3; ++i) kk[i] = 160 * hw + 64 * i + (i < 2 ? lane : (lane & 31));
    const u32 vm_ = (u32)(r2 * 3840 + c8) * 2, vq_ = (u32)(r2 * 4096 + c8) * 2;
    const u64 mb = (u64)wmod + ((u64)160 * hw * 3840 + mblk * 16) * 2;
    asm volatile("global_load_dwordx2 %0, %1, %2" : "=&v"(cv) : "v"((u32)k0 * 2), "s"((u64)cnd) : "memory");
    if (crit) {
#pragma unroll
      for (int i = 0; i < 5; ++i) dma16(vm_, mb + (u64)i * 32 * 3840 * 2, lds0 + L_WMOD + (hw * 5 + i) * 1024);
      touch(WS_MOD, wsb, lds0 + L_OB + hw * 256);
    }
    wg_barrier();
    const int chunk = mblk / 40, mcol = (mblk % 40) * 16 + (lane & 15);
    const u32 mslot = (u32)((chunk < 2 ? 0 : (chunk == 3 || chunk == 4) ? 1 : 2) * 1280 + 2 * mcol + ((chunk == 1 || chunk >= 4) ? 1 : 0));
    auto mod_gemv = [&]() {
      f32x4 acc = {0.f, 0.f, 0.f, 0.f};
#pragma unroll
      for (int i = 0; i < 5; ++i) acc = mfma(a_frag(sm, L_SC + (160 * hw + 32 * i) * 2, lane), tr_frag(sm, L_WMOD + (hw * 5 + i) * 1024, lane), acc);
      if (lane < 16) lds_st<float>(sm, L_RED + (hw * 16 + lane) * 4, acc.x);
      lds_arrive(sm, F_C1, lane);
      if (hw == 0) {
        lds_wait(sm, F_C1, 4);
        const int col = lane & 15;
        const float v = lds_ld<float>(sm, L_RED + col * 4) + lds_ld<float>(sm, L_RED + (16 + col) * 4) + lds_ld<float>(sm, L_RED + (32 + col) * 4) + lds_ld<float>(sm, L_RED + (48 + col) * 4);
        const u32 v16 = pack_bf(v, v) & 0xffffu;
        const u32 o = WS_MOD + (u32)(((lane >> 4) * 3840 + mslot) * 8);
        put16(ws, o, v16, tag); put16(ws, o + 4 * 3840 * 8, v16, tag);
      }
    };
    if (crit) {
      asm volatile("s_waitcnt vmcnt(6)" : "+v"(cv) :: "memory");
      lds_st<u32x2>(sm, L_SC + k0 * 2, u32x2{pack_bf(siluf(bf_lo(cv.x)), siluf(bf_hi(cv.x))), pack_bf(siluf(bf_lo(cv.y)), siluf(bf_hi(cv.y)))});
      vmn<1>(); lgkm0();
      HSTAMP(13);
      mod_gemv();
      HSTAMP(14);
    }
#pragma unroll
    for (int i = 0; i < 5; ++i) asm volatile("global_load_dword %0, %1, %2 offset:%3" : "=&v"(xp[i]) : "v"((u32)lane * 4), "s"((u64)x), "i"(256 * i) : "memory");
#pragma unroll
    for (int i = 0; i < 3; ++i) asm volatile("global_load_ushort %0, %1, %2" : "=&v"(xk[i]) : "v"((u32)kk[i] * 2), "s"((u64)x) : "memory");
    {
      const u64 qb = (u64)wq + ((u64)160 * hw * 4096 + head * 128 + split * 16) * 2;
#pragma unroll
      for (int i = 0; i < 5; ++i) dma16(vq_, qb + (u64)i * 32 * 4096 * 2, lds0 + L_WQ + (hw * 5 + i) * 1024);
    }
    asm volatile("s_waitcnt vmcnt(5)" : "+v"(cv), "+v"(xp[0]), "+v"(xp[1]), "+v"(xp[2]), "+v"(xp[3]), "+v"(xp[4]), "+v"(xk[0]), "+v"(xk[1]), "+v"(xk[2]) :: "memory");
    const LN n1 = ln_stats(xp);
    if (is_mod && !crit) lds_st<u32x2>(sm, L_SC + k0 * 2, u32x2{pack_bf(siluf(bf_lo(cv.x)), siluf(bf_hi(cv.x))), pack_bf(siluf(bf_lo(cv.y)), siluf(bf_hi(cv.y)))});
    HSTAMP(12);
    vmn<0>();
    STAMP(1);
    bf16x8 bq[5];
#pragma unroll
    for (int i = 0; i < 5; ++i) bq[i] = tr_frag(sm, L_WQ + (hw * 5 + i) * 1024, lane);
    const u64 mrep = wsb + WS_MOD + (u64)xcd * 3840 * 8;
    {
      u32x4 g[3];
      const u32 vo[3] = {(u32)kk[0] * 16, (u32)kk[1] * 16, (u32)kk[2] * 16};
      poll<3>(g, vo, mrep, tag);
      STAMP(2);
#pragma unroll
      for (int i = 0; i < 3; ++i) {
        const float h = ln_mod(__uint_as_float(xk[i] << 16), n1, rbf(1.f + __uint_as_float(g[i].z << 16)), __uint_as_float(g[i].x << 16));
        lds_st<u16>(sm, L_H + kk[i] * 2, (u16)(__float_as_uint(h) >> 16));
      }
      lgkm0();
      f32x4 acc = {0.f, 0.f, 0.f, 0.f};
#pragma unroll
      for (int i = 0; i < 5; ++i) acc = mfma(a_frag(sm, L_H + (160 * hw + 32 * i) * 2, lane), bq[i], acc);
      if (lane < 16) lds_st<float>(sm, L_RED + 256 + (hw * 16 + lane) * 4, acc.x);
      lds_arrive(sm, F_C2, lane);
    }
    if (hw == 0) {
      lds_wait(sm, F_C2, 4);
      const int col = lane & 15;
      const float v = lds_ld<float>(sm, L_RED + 256 + col * 4) + lds_ld<float>(sm, L_RED + 256 + (16 + col) * 4) + lds_ld<float>(sm, L_RED + 256 + (32 + col) * 4) + lds_ld<float>(sm, L_RED + 256 + (48 + col) * 4);
      if (lane < 16) put16(ws, WS_Q + (u32)(head * 128 + split * 16 + lane) * 8, pack_bf(v, v) & 0xffffu, tag);
      STAMP(3);
      u32x4 g[1];
      const u32 vo[1] = {(u32)lane * 16};
      poll<1>(g, vo, wsb + WS_Q + (u64)head * 1024, tag);
      lds_st<u32>(sm, L_Q + lane * 4, payload(g[0]));
      lgkm0();
      if (lane == 0) *(volatile LAS u32*)(sm + F_Q) = 1u;
      STAMP(4);
    } else {
      lds_wait(sm, F_Q, 1);
    }
    if (is_mod && !crit) {
#pragma unroll
      for (int i = 0; i < 5; ++i) dma16(vm_, mb + (u64)i * 32 * 3840 * 2, lds0 + L_WMOD + (hw * 5 + i) * 1024);
      vmn<0>(); lgkm0();
      mod_gemv();
    }
    {
      const u32 vo = (u32)(r2 * 640 + c8) * 2;
      const u64 b = (u64)wo + ((u64)head * 128 * 640 + split * 80) * 2;
#pragma unroll
      for (int i = 0; i < 5; ++i) {
        const int id = 5 * hw + i;
        dma16(vo, b + (u64)((32 * (id & 3)) * 640 + (id >> 2) * 16) * 2, lds0 + L_WO + id * 1024);
      }
      vmn<0>();
      if (!is_d) {
#pragma unroll
        for (int i = 0; i < 5; ++i) dma16g((lane & 1 ? wu : wg) + fj * 8 + (size_t)(160 * hw + 32 * i + r2) * FF, lds0 + L_WF + (5 * hw + i) * 1024);
      } else {
        const u64 bd_ = (u64)wd + dj * 32;
#pragma unroll
        for (int i = 0; i < 7; ++i) { const int s_ = 14 * hw + i; dma16(vo, bd_ + (u64)s_ * 32 * 640 * 2, lds0 + L_WF + s_ * 1024); }
        vmn<0>();
#pragma unroll
        for (int i = 7; i < 14; ++i) { const int s_ = 14 * hw + i; if (s_ < 54) dma16(vo, bd_ + (u64)s_ * 32 * 640 * 2, lds0 + L_WF + s_ * 1024); }
      }
      vmn<0>();
    }
    STAMP(5);
    lds_wait(sm, F_LATE, 1);
    if (!is_d) {
      u32x4 g[3];
      const u32 vo[3] = {(u32)kk[0] * 16 + 10240, (u32)kk[1] * 16 + 10240, (u32)kk[2] * 16 + 10240};
      poll<3>(g, vo, mrep, tag, 1);
#pragma unroll
      for (int i = 0; i < 3; ++i) {
        lds_st<u16>(sm, L_SB + kk[i] * 2, (u16)g[i].x);
        lds_st<u16>(sm, L_CB1 + kk[i] * 2, (u16)pack_bf(1.f + __uint_as_float(g[i].z << 16), 0.f));
      }
    } else if (hw == 1) {
      const int col = lane & 15;
      u32x4 g[1];
      const u32 vo[1] = {(u32)(20480 + (dj * 16 + col) * 16)};
      const u32 xc = ((const GAS u16*)x)[dj * 16 + col];
      poll<1>(g, vo, mrep, tag, 1);
      if (lane < 16) {
        lds_st<float>(sm, L_D16 + 64 + lane * 4, __uint_as_float(g[0].x << 16));
        lds_st<float>(sm, L_D16 + 128 + lane * 4, __uint_as_float(g[0].z << 16));
        lds_st<float>(sm, L_D16 + lane * 4, __uint_as_float(xc << 16));
      }
    }
  }

  wg_barrier();
  STAMP(6);
  const u64 a0blk = wsb + WS_A0 + (u64)head * 8 * 144 * 16;
  if (wave < 3) {
    const f32x2 p0 = lds_ld<f32x2>(sm, L_ML), p1 = lds_ld<f32x2>(sm, L_ML + 8), p2 = lds_ld<f32x2>(sm, L_ML + 16), p3 = lds_ld<f32x2>(sm, L_ML + 24);
    const float M = fmaxf(fmaxf(p0.x, p1.x), fmaxf(p2.x, p3.x));
    const float e0 = __expf(p0.x - M), e1 = __expf(p1.x - M), e2 = __expf(p2.x - M), e3 = __expf(p3.x - M);
    if (wave < 2) {
      const int d = tid, c = d >> 3, i = d & 7;
      const u32 o = L_ACC + (u32)(((i >> 2) * 16 + c) * 4 + (i & 3)) * 4;
      const float v = e0 * (lds_ld<float>(sm, o) + lds_ld<float>(sm, o + 512)) + e1 * (lds_ld<float>(sm, o + 1024) + lds_ld<float>(sm, o + 1536))
                    + e2 * (lds_ld<float>(sm, o + 2048) + lds_ld<float>(sm, o + 2560)) + e3 * (lds_ld<float>(sm, o + 3072) + lds_ld<float>(sm, o + 3584));
      put32(a0blk, (u32)(split * 144 + d) * 16, __float_as_uint(v), tag);
    } else if (lane < 2) {
      const float L = e0 * p0.y + e1 * p1.y + e2 * p2.y + e3 * p3.y;
      put32(a0blk, (u32)(split * 144 + 128 + lane) * 16, __float_as_uint(lane ? L : M), tag);
    }
  }
  TSTAMP(12);
  bf16x8 bo[4];
  if (wave < 5) {
#pragma unroll
    for (int t = 0; t < 4; ++t) bo[t] = tr_frag(sm, L_WO + (wave * 4 + t) * 1024, lane);
  }
  {
    u32x4 g[3];
    const u32 vo[3] = {(u32)(wave * 144 + lane) * 16, (u32)(wave * 144 + 64 + lane) * 16, (u32)(wave * 144 + 128 + (lane & 1)) * 16};
    poll<3>(g, vo, a0blk, tag);
    lds_st<u32>(sm, L_O + (wave * 128 + lane) * 4, payload(g[0]));
    lds_st<u32>(sm, L_O + (wave * 128 + 64 + lane) * 4, payload(g[1]));
    if (lane < 2) lds_st<u32>(sm, L_OML + (wave * 2 + lane) * 4, payload(g[2]));
  }
  STAMP(7);
  wg_barrier();
  TSTAMP(13);
  const u32 stage = L_OB + (u32)wave * 512;
  if (wave < 5) {
    const f32x2 ml = lds_ld<f32x2>(sm, L_OML + (lane & 7) * 8);
    float M = ml.x;
    M = fmaxf(M, dpp<XOR1>(M)); M = fmaxf(M, dpp<XOR2>(M)); M = fmaxf(M, dpp<HMIR>(M));
    const float e = __expf(ml.x - M);
    float L = e * ml.y;
    L += dpp<XOR1>(L); L += dpp<XOR2>(L); L += dpp<HMIR>(L);
    const float wn = e / L;
    float o0 = 0.f, o1 = 0.f;
#pragma unroll
    for (int s = 0; s < 8; ++s) {
      const float ws_ = __uint_as_float((u32)__builtin_amdgcn_readlane((int)__float_as_uint(wn), s));
      const f32x2 v = lds_ld<f32x2>(sm, L_O + (s * 128 + 2 * lane) * 4);
      o0 += ws_ * v.x; o1 += ws_ * v.y;
    }
    lds_st<u32>(sm, stage + lane * 4, pack_bf(o0, o1));
    lgkm0();
    f32x4 acc = {0.f, 0.f, 0.f, 0.f};
#pragma unroll
    for (int t = 0; t < 4; ++t) acc = mfma(a_frag(sm, stage + t * 64, lane), bo[t], acc);
    if (lane < 16) put32(wsb + WS_A, (u32)(((split * 5 + wave) * 32 + head) * 16 + lane) * 16, __float_as_uint(acc.x), tag);
  }
  STAMP(8);
  if (!is_d) {
    if (wave < 5) {
      bf16x8 bf[4];
#pragma unroll
      for (int t = 0; t < 4; ++t) bf[t] = tr_frag(sm, L_WF + (wave * 4 + t) * 1024, lane);
      const u32 cb1 = lds_ld<u32>(sm, L_CB1 + (wave * 64 + lane) * 4), sb = lds_ld<u32>(sm, L_SB + (wave * 64 + lane) * 4);
      u32x4 g[1];
      const u32 vo[1] = {(u32)(wave * 64 + lane) * 16};
      poll<1>(g, vo, wsb + WS_B + (u64)xcd * 640 * 8, tag);
      const u32 own = payload(g[0]);
      lds_st<u32>(sm, L_X2 + (wave * 64 + lane) * 4, own);
      STAMP(9);
      wg_barrier();
      TSTAMP(14);
      u32 xp[5];
#pragma unroll
      for (int i = 0; i < 5; ++i) xp[i] = lds_ld<u32>(sm, L_X2 + (i * 64 + lane) * 4);
      const LN n2 = ln_stats(xp);
      lds_st<u32>(sm, stage + lane * 4, pack_bf(ln_mod(bf_lo(own), n2, bf_lo(cb1), bf_lo(sb)), ln_mod(bf_hi(own), n2, bf_hi(cb1), bf_hi(sb))));
      lgkm0();
      f32x4 acc = {0.f, 0.f, 0.f, 0.f};
#pragma unroll
      for (int t = 0; t < 4; ++t) acc = mfma(a_frag(sm, stage + t * 64, lane), bf[t], acc);
      if (lane < 16) lds_st<float>(sm, L_P + (wave * 16 + lane) * 4, acc.x);
    } else {
      wg_barrier();
    }
    wg_barrier();
    TSTAMP(15);
    if (wave == 0) {
      const int col = lane & 15;
      float v = 0.f;
#pragma unroll
      for (int w = 0; w < 5; ++w) v += lds_ld<float>(sm, L_P + (w * 16 + col) * 4);
      v = rbf(v);
      const float u = dpp<ROR8>(v);
      const float act = rbf(rbf(siluf(v)) * u);
      if (col < 8) put16(ws, WS_C + (u32)((lane >> 4) * 1792 + fj * 8 + col) * 8, __float_as_uint(act) >> 16, tag);
    }
    STAMP(10);
  } else {
    if (wave == 7) {
      u32x4 g[8];
      u32 vo[8];
#pragma unroll
      for (int i = 0; i < 8; ++i) vo[i] = (u32)(((dj * 32 + 4 * i + (lane >> 4)) * 16 + (lane & 15)) * 16);
      poll<8>(g, vo, wsb + WS_A, tag);
      float y = 0.f;
#pragma unroll
      for (int i = 0; i < 8; ++i) y += __uint_as_float(payload(g[i]));
      y = rbf(xrow_sum(y));
      const int col = lane & 15;
      const float x2 = rbf(lds_ld<float>(sm, L_D16 + col * 4) + rbf(lds_ld<float>(sm, L_D16 + 64 + col * 4) * y));
      const u32 o = WS_B + (u32)((lane >> 4) * 640 + dj * 16 + col) * 8;
      put16(ws, o, __float_as_uint(x2) >> 16, tag); put16(ws, o + 4 * 640 * 8, __float_as_uint(x2) >> 16, tag);
      if (lane < 16) lds_st<float>(sm, L_D16 + 192 + lane * 4, x2);
      STAMP(9);
    }
    const int nst = wave == 7 ? 5 : 7;
    bf16x8 bd[7];
#pragma unroll
    for (int t = 0; t < 7; ++t) bd[t] = tr_frag(sm, L_WF + (wave * 7 + (t < nst ? t : 0)) * 1024, lane);
    wg_barrier();
    {
      const int ng = nst * 16;
      const int i1 = lane + 64 < ng ? lane + 64 : lane;
      u32x4 g[2];
      const u32 vo[2] = {(u32)(wave * 112 + lane) * 16, (u32)(wave * 112 + i1) * 16};
      poll<2>(g, vo, wsb + WS_C + (u64)(dj & 3) * 1792 * 8, tag);
      STAMP(10);
      lds_st<u32>(sm, stage + lane * 4, payload(g[0]));
      lds_st<u32>(sm, stage + i1 * 4, payload(g[1]));
      lgkm0();
      f32x4 acc = {0.f, 0.f, 0.f, 0.f};
#pragma unroll
      for (int t = 0; t < 7; ++t) if (t < nst) acc = mfma(a_frag(sm, stage + t * 64, lane), bd[t], acc);
      if (lane < 16) lds_st<float>(sm, L_P + (wave * 16 + lane) * 4, acc.x);
    }
    wg_barrier();
    if (wave == 0) {
      const int col = lane & 15;
      float v = 0.f;
#pragma unroll
      for (int w = 0; w < 8; ++w) v += lds_ld<float>(sm, L_P + (w * 16 + col) * 4);
      const float o = lds_ld<float>(sm, L_D16 + 192 + col * 4) + rbf(lds_ld<float>(sm, L_D16 + 128 + col * 4) * rbf(v));
      const u32 pr = pack_bf(o, dpp<XOR1>(o));
      if (lane < 16 && !(lane & 1)) ((GAS u32*)out)[dj * 8 + (lane >> 1)] = pr;
      if (dj == 0 && lane == 0) gst(ws, WS_EPOCH, next);
      if (dj == 0) {
        const u64 te = __builtin_amdgcn_s_memrealtime();
        u64 X = PROBE == 0 ? 0 : PROBE == 1 ? pt1 - pt0 : PROBE == 2 ? pt2 - pt0 : PROBE == 3 ? pt3 - pt0 : te - pt0;
        if (X > 10000) X = 10000;
        for (int i = 0; i < 200000; ++i) { if (__builtin_amdgcn_s_memrealtime() - te >= X) break; }
      }
    }
    STAMP(11);
  }
  STAMP_FLUSH();
}

extern "C" void kernel_launch(void* const* d_in, const int* in_sizes, int n_in, void* d_out, int out_size, void* d_ws, size_t ws_size, hipStream_t stream) {
  fused_decode_kernel<<<256, 512, 0, stream>>>((const u16*)d_in[0], (const u16*)d_in[1], (const u16*)d_in[2], (const u16*)d_in[3], (const u16*)d_in[4],
                                               (const u16*)d_in[6], (const u16*)d_in[7], (const u16*)d_in[8], (const u16*)d_in[9], (const u16*)d_in[10],
                                               (u16*)d_out, (unsigned char*)d_ws);
}
```

```cpp
#include <hip/hip_runtime.h>
#include <hip/hip_bf16.h>
#include <stdint.h>

typedef unsigned short u16;
typedef unsigned int u32;
typedef unsigned long long u64;
typedef u32 u32x4 __attribute__((ext_vector_type(4)));
typedef u32 u32x2 __attribute__((ext_vector_type(2)));
typedef float f32x4 __attribute__((ext_vector_type(4)));
typedef float f32x2 __attribute__((ext_vector_type(2)));
typedef __bf16 bf16x8 __attribute__((ext_vector_type(8)));
typedef __bf16 bf16x2 __attribute__((ext_vector_type(2)));
typedef short s16x4 __attribute__((ext_vector_type(4)));
typedef short s16x8 __attribute__((ext_vector_type(8)));
#define GAS __attribute__((address_space(1)))
#define LAS __attribute__((address_space(3)))
#define RLX_AGENT __ATOMIC_RELAXED, __HIP_MEMORY_SCOPE_AGENT

constexpr int SK = 16384, FF = 1728;
constexpr u32 SPIN_CAP = 1u << 16;
constexpr u32 PK = 1990;
constexpr u32 PNB = 8, PSLOW = 200;
constexpr u32 PT0 = 35;
constexpr u32 PHW = 32, PHB = 32;
constexpr u32 PW0 = 2010, PW = 130, PWM = 76;
constexpr u32 PF0 = 3270, PF = 130;
constexpr u32 PMP = 450, PQP = 850;
constexpr int MERGE_AT = 23;

constexpr int RB = 8;
constexpr u32 WS_EPOCH = 0;
constexpr u32 WS_MOD = 4096;
constexpr u32 WS_MP = WS_MOD + 8 * 3840 * 8;
constexpr u32 WS_Q = WS_MP + 4 * 8 * 640 * 8;
constexpr u32 WS_A0 = WS_Q + 32 * 8 * 128 * 8;
constexpr u32 WS_A = WS_A0 + 256 * 144 * 8;
constexpr u32 WS_B = WS_A + 40 * 32 * 16 * 8;
constexpr u32 WS_C = WS_B + RB * 640 * 8;

constexpr u32 L_WMOD = 0, L_WQ = 20480;
constexpr u32 L_KV = 40960;
constexpr u32 L_WF = L_KV, L_WD = L_WF + 20480;
constexpr u32 L_WM = 0;
constexpr u32 L_SC = L_KV + 96 * 1024;
constexpr u32 L_WO = L_SC - 20480;
constexpr u32 L_H = L_SC + 1280;
constexpr u32 L_CB1 = L_H + 1280;
constexpr u32 L_SB = L_CB1 + 1280;
constexpr u32 L_Q = L_SB + 1280;
constexpr u32 L_RED = L_Q + 256;
constexpr u32 L_ACC = L_RED + 512;
constexpr u32 L_ML = L_ACC + 4096;
constexpr u32 L_O = L_ML + 64;
constexpr u32 L_OML = L_O + 4096;
constexpr u32 L_OB = L_OML + 64;
constexpr u32 L_HACC = L_O, L_HL = L_OB + 1024, L_HM = L_OB + 2048;
constexpr u32 L_X2 = L_OB + 4096;
constexpr u32 L_P = L_X2 + 1280;
constexpr u32 L_D16 = L_P + 512;
constexpr u32 L_FLAG = L_D16 + 256;
constexpr u32 LDS_BYTES = L_FLAG + 64;

__device__ __forceinline__ float bf_lo(u32 p) { return __uint_as_float(p << 16); }
__device__ __forceinline__ float bf_hi(u32 p) { return __uint_as_float(p & 0xffff0000u); }
__device__ __forceinline__ u32 pack_bf(float lo, float hi) {
  f32x2 v = {lo, hi};
  return __builtin_bit_cast(u32, __builtin_convertvector(v, bf16x2));
}
__device__ __forceinline__ float rbf(float f) { return bf_lo(pack_bf(f, f)); }
__device__ __forceinline__ float siluf(float x) { return x * __builtin_amdgcn_rcpf(1.f + __expf(-x)); }
template <int CTRL> __device__ __forceinline__ float dpp(float x) {
  return __builtin_bit_cast(float, __builtin_amdgcn_mov_dpp(__builtin_bit_cast(int, x), CTRL, 0xf, 0xf, true));
}
template <int CTRL> __device__ __forceinline__ u32 dppu(u32 x) { return (u32)__builtin_amdgcn_mov_dpp((int)x, CTRL, 0xf, 0xf, true); }
constexpr int XOR1 = 0xB1, XOR2 = 0x4E, HMIR = 0x141, MIR = 0x140, ROR8 = 0x128, BCAST = 0x150;
__device__ __forceinline__ float xrow_sum(float x) {
  auto s = __builtin_amdgcn_permlane16_swap(__float_as_uint(x), __float_as_uint(x), false, false);
  x = __uint_as_float(s[0]) + __uint_as_float(s[1]);
  auto t = __builtin_amdgcn_permlane32_swap(__float_as_uint(x), __float_as_uint(x), false, false);
  return __uint_as_float(t[0]) + __uint_as_float(t[1]);
}
__device__ __forceinline__ float xrow_max(float x) {
  auto s = __builtin_amdgcn_permlane16_swap(__float_as_uint(x), __float_as_uint(x), false, false);
  x = fmaxf(__uint_as_float(s[0]), __uint_as_float(s[1]));
  auto t = __builtin_amdgcn_permlane32_swap(__float_as_uint(x), __float_as_uint(x), false, false);
  return fmaxf(__uint_as_float(t[0]), __uint_as_float(t[1]));
}
__device__ __forceinline__ float wave_sum(float x) {
  x += dpp<XOR1>(x); x += dpp<XOR2>(x); x += dpp<HMIR>(x); x += dpp<MIR>(x);
  return xrow_sum(x);
}
__device__ __forceinline__ float wave_max(float x) {
  x = fmaxf(x, dpp<XOR1>(x)); x = fmaxf(x, dpp<XOR2>(x)); x = fmaxf(x, dpp<HMIR>(x)); x = fmaxf(x, dpp<MIR>(x));
  return xrow_max(x);
}
__device__ __forceinline__ void lgkm0() { asm volatile("s_waitcnt lgkmcnt(0)" ::: "memory"); }
__device__ __forceinline__ void vm0() { asm volatile("s_waitcnt vmcnt(0)" ::: "memory"); }
__device__ __forceinline__ void wg_barrier() { lgkm0(); __builtin_amdgcn_s_barrier(); asm volatile("" ::: "memory"); }
__device__ __forceinline__ void gst(unsigned char* ws, u32 off, u64 v) { __hip_atomic_store((GAS u64*)(ws + off), v, RLX_AGENT); }

template <typename T> __device__ __forceinline__ T lds_ld(LAS unsigned char* s, u32 off) { return *(LAS T*)(s + off); }
template <typename T> __device__ __forceinline__ void lds_st(LAS unsigned char* s, u32 off, T v) { *(LAS T*)(s + off) = v; }
__device__ __forceinline__ void lds_wait(LAS unsigned char* s, u32 off, u32 target) {
#pragma nounroll
  for (u32 n = 0; n < (SPIN_CAP << 4); ++n) {
    const u32 v = (u32)__builtin_amdgcn_readfirstlane((int)*(volatile LAS u32*)(s + off));
    if (v >= target) break;
    __builtin_amdgcn_s_sleep(1);
  }
  asm volatile("" ::: "memory");
}
__device__ __forceinline__ void lds_arrive(LAS unsigned char* s, u32 off, int lane) {
  lgkm0();
  if (lane == 0) __hip_atomic_fetch_add((LAS u32*)(s + off), 1u, __ATOMIC_RELAXED, __HIP_MEMORY_SCOPE_WORKGROUP);
}
__device__ __forceinline__ void dma16(u32 voff, u64 base, u32 lds) {
  u32 keep;
  lds = (u32)__builtin_amdgcn_readfirstlane((int)lds);
  asm volatile("s_mov_b32 %0, m0\n\ts_mov_b32 m0, %3\n\ts_nop 0\n\tglobal_load_lds_dwordx4 %1, %2 nt\n\ts_mov_b32 m0, %0"
               : "=&s"(keep) : "v"(voff), "s"(base), "s"(lds) : "memory");
}
__device__ __forceinline__ void dma16g(const u16* g, u32 lds) {
  u32 keep;
  lds = (u32)__builtin_amdgcn_readfirstlane((int)lds);
  asm volatile("s_mov_b32 %0, m0\n\ts_mov_b32 m0, %2\n\ts_nop 0\n\tglobal_load_lds_dwordx4 %1, off nt\n\ts_mov_b32 m0, %0"
               : "=&s"(keep) : "v"(g), "s"(lds) : "memory");
}
__device__ __forceinline__ void touch(u32 voff, u64 base, u32 lds) {
  u32 keep;
  asm volatile("s_mov_b32 %0, m0\n\ts_mov_b32 m0, %3\n\ts_nop 0\n\tglobal_load_lds_dword %1, %2\n\ts_mov_b32 m0, %0"
               : "=&s"(keep) : "v"(voff), "s"(base), "s"(lds) : "memory");
}
template <int N> __device__ __forceinline__ void vmn() { asm volatile("s_waitcnt vmcnt(%0)" :: "i"(N) : "memory"); }
struct Tag { u32 lo, hi; };
__device__ __forceinline__ bool tag_ok(const u32x4& g, const Tag& t) {
  return ((((g.x ^ t.lo) | (g.z ^ t.lo)) >> 24) | (g.y ^ t.hi) | (g.w ^ t.hi)) == 0;
}
__device__ __forceinline__ u32 f24(float v) { return (__float_as_uint(v) + 0x80u) >> 8; }
__device__ __forceinline__ float v24(u32 g) { return __uint_as_float(g << 8); }
__device__ __forceinline__ u32 payload(const u32x4& g) { return __builtin_amdgcn_perm(g.z, g.x, 0x05040100u); }
__device__ __forceinline__ void put16(unsigned char* ws, u32 off, u32 v16, const Tag& t) { gst(ws, off, ((u64)t.hi << 32) | t.lo | v16); }
__device__ __forceinline__ void put2(u64 base, u32 voff, u32 a, u32 b, const Tag& t) {
  const u32x4 g = {a | t.lo, t.hi, b | t.lo, t.hi};
  asm volatile("global_store_dwordx4 %0, %1, %2 sc1\n\ts_nop 1" :: "v"(voff), "v"(g), "s"(base) : "memory");
}
__device__ __forceinline__ void gld2_again(u32x4& r, u32 voff, u64 base, u64 mask) {
  u64 keep;
  asm volatile("s_mov_b64 %1, exec\n\ts_mov_b64 exec, %4\n\tglobal_load_dwordx4 %0, %2, %3 sc1\n\ts_mov_b64 exec, %1"
               : "+v"(r), "=&s"(keep) : "v"(voff), "s"(base), "s"(mask) : "memory");
}
template <int N> __device__ __forceinline__ void poll(u32x4 (&g)[N], const u32 (&voff)[N], u64 base, const Tag& t, int nap = 0) {
  u64 miss[N];
#pragma unroll
  for (int i = 0; i < N; ++i) { miss[i] = __builtin_amdgcn_ballot_w64(true); asm volatile("" : "=v"(g[i])); }
#pragma nounroll
  for (u32 n = 0; n < SPIN_CAP; ++n) {
#pragma unroll
    for (int i = 0; i < N; ++i) gld2_again(g[i], voff[i], base, miss[i]);
    asm volatile("s_waitcnt vmcnt(0)" ::: "memory");
    u64 any = 0;
#pragma unroll
    for (int i = 0; i < N; ++i) { asm volatile("" : "+v"(g[i])); miss[i] = __builtin_amdgcn_ballot_w64(!tag_ok(g[i], t)); any |= miss[i]; }
    if (any == 0) break;
    if (nap) __builtin_amdgcn_s_sleep(8);
  }
}
__device__ __forceinline__ bf16x8 tr_frag(LAS unsigned char* s, u32 off, int lane) {
  const u32 a = off + (u32)((8 * (lane >> 4) + ((lane >> 2) & 3)) * 32 + (lane & 3) * 8);
  const s16x4 r0 = __builtin_amdgcn_ds_read_tr16_b64_v4i16((LAS s16x4*)(s + a));
  const s16x4 r1 = __builtin_amdgcn_ds_read_tr16_b64_v4i16((LAS s16x4*)(s + a + 128));
  const s16x8 r = __builtin_shufflevector(r0, r1, 0, 1, 2, 3, 4, 5, 6, 7);
  return __builtin_bit_cast(bf16x8, r);
}
template <int PITCH> __device__ __forceinline__ bf16x8 tr_frag_p(LAS unsigned char* s, u32 off, int lane) {
  const u32 a = off + (u32)((8 * (lane >> 4) + ((lane >> 2) & 3)) * PITCH + (lane & 3) * 8);
  const s16x4 r0 = __builtin_amdgcn_ds_read_tr16_b64_v4i16((LAS s16x4*)(s + a));
  const s16x4 r1 = __builtin_amdgcn_ds_read_tr16_b64_v4i16((LAS s16x4*)(s + a + 4 * PITCH));
  const s16x8 r = __builtin_shufflevector(r0, r1, 0, 1, 2, 3, 4, 5, 6, 7);
  return __builtin_bit_cast(bf16x8, r);
}
template <int PITCH> __device__ __forceinline__ s16x4 tr_frag16(LAS unsigned char* s, u32 off, int lane) {
  return __builtin_amdgcn_ds_read_tr16_b64_v4i16((LAS s16x4*)(s + off + (u32)((4 * (lane >> 4) + ((lane >> 2) & 3)) * PITCH + (lane & 3) * 8)));
}
__device__ __forceinline__ s16x4 a_frag16(LAS unsigned char* s, u32 off, int lane) { return lds_ld<s16x4>(s, off + (u32)(lane >> 4) * 8); }
__device__ __forceinline__ f32x4 mfma16(s16x4 a, s16x4 b, f32x4 c) { return __builtin_amdgcn_mfma_f32_16x16x16bf16_1k(a, b, c, 0, 0, 0); }
__device__ __forceinline__ bf16x8 a_frag(LAS unsigned char* s, u32 off, int lane) {
  return __builtin_bit_cast(bf16x8, lds_ld<u32x4>(s, off + (u32)(lane >> 4) * 16));
}
__device__ __forceinline__ f32x4 mfma(bf16x8 a, bf16x8 b, f32x4 c) { return __builtin_amdgcn_mfma_f32_16x16x32_bf16(a, b, c, 0, 0, 0); }

struct Tile { u32x4 k[4], v[4]; };
template <int OFF> __device__ __forceinline__ void ldnt(u32x4& r, u32 voff, u64 base) {
  asm volatile("global_load_dwordx4 %0, %1, %2 offset:%3 nt" : "+v"(r) : "v"(voff), "s"(base), "i"(OFF));
}
__device__ __forceinline__ void issue(Tile& t, u32 voff, u64 kb, u64 vb) {
  ldnt<0>(t.k[0], voff, kb); ldnt<1024>(t.k[1], voff, kb); ldnt<2048>(t.k[2], voff, kb); ldnt<3072>(t.k[3], voff, kb);
  ldnt<0>(t.v[0], voff, vb); ldnt<1024>(t.v[1], voff, vb); ldnt<2048>(t.v[2], voff, vb); ldnt<3072>(t.v[3], voff, vb);
}
__device__ __forceinline__ void issue_lds(u32 voff, u64 kb, u64 vb, u32 lds) {
  u32 keep;
  asm volatile("s_mov_b32 %0, m0\n\ts_mov_b32 m0, %4\n\ts_nop 0\n\t"
               "global_load_lds_dwordx4 %1, %2 nt\n\tglobal_load_lds_dwordx4 %1, %2 offset:1024 nt\n\t"
               "global_load_lds_dwordx4 %1, %2 offset:2048 nt\n\tglobal_load_lds_dwordx4 %1, %2 offset:3072 nt\n\t"
               "s_add_u32 m0, m0, 0x1000\n\ts_nop 0\n\t"
               "global_load_lds_dwordx4 %1, %3 nt\n\tglobal_load_lds_dwordx4 %1, %3 offset:1024 nt\n\t"
               "global_load_lds_dwordx4 %1, %3 offset:2048 nt\n\tglobal_load_lds_dwordx4 %1, %3 offset:3072 nt\n\t"
               "s_mov_b32 m0, %0"
               : "=&s"(keep) : "v"(voff), "s"(kb), "s"(vb), "s"(lds) : "memory", "scc");
}
__device__ __forceinline__ void fetch_lds(Tile& t, LAS unsigned char* s, u32 off) {
#pragma unroll
  for (int u = 0; u < 4; ++u) { t.k[u] = *(LAS u32x4*)(s + off + u * 1024); t.v[u] = *(LAS u32x4*)(s + off + 4096 + u * 1024); }
}
__device__ __forceinline__ void until(u32 due) {
#pragma nounroll
  for (u32 n = 0; n < SPIN_CAP; ++n) {
    if ((int)((u32)__builtin_amdgcn_s_memrealtime() - due) >= 0) break;
    __builtin_amdgcn_s_sleep(1);
  }
}
__device__ __forceinline__ void pin(Tile& t) {
  asm volatile("" : "+v"(t.k[0]), "+v"(t.k[1]), "+v"(t.k[2]), "+v"(t.k[3]), "+v"(t.v[0]), "+v"(t.v[1]), "+v"(t.v[2]), "+v"(t.v[3]));
}
struct Attn { float m, l; f32x4 acc; u32x4 q; u32 sel[4]; bool lo8, b4; };
template <int N> __device__ __forceinline__ void wait_but(Attn& a) {
  asm volatile("s_waitcnt vmcnt(%2)" : "+v"(a.acc), "+v"(a.l) : "i"(8 * N) : "memory");
}
__device__ __forceinline__ float dot2(u32 a, u32 b, float c) {
  return __builtin_amdgcn_fdot2_f32_bf16(__builtin_bit_cast(bf16x2, a), __builtin_bit_cast(bf16x2, b), c, false);
}
__device__ __forceinline__ void attn_init(Attn& a, int lane) {
  a.m = -INFINITY; a.l = 0.f; a.acc = f32x4{0.f, 0.f, 0.f, 0.f};
  const int i = lane & 15;
  a.lo8 = i < 8; a.b4 = (i & 4) != 0;
  const u32 src = i < 8 ? 0x0302u : 0x0100u;
  const u32 one = (i & 1) ? ((src << 16) | 0x0c0cu) : (0x0c0c0000u | src);
#pragma unroll
  for (int r = 0; r < 4; ++r) a.sel[r] = (((i & 7) >> 1) == r) ? one : 0x0c0c0c0cu;
}
__device__ __forceinline__ void consume(const Tile& t, Attn& a) {
  float s[4];
#pragma unroll
  for (int u = 0; u < 4; ++u) s[u] = dot2(t.k[u].w, a.q.w, dot2(t.k[u].z, a.q.z, dot2(t.k[u].y, a.q.y, dot2(t.k[u].x, a.q.x, 0.f))));
  const float a0 = s[0] + dpp<ROR8>(s[0]), a1 = s[1] + dpp<ROR8>(s[1]), a2 = s[2] + dpp<ROR8>(s[2]), a3 = s[3] + dpp<ROR8>(s[3]);
  const float t01 = a.lo8 ? a0 : a1, t23 = a.lo8 ? a2 : a3;
  const float wa = t01 + dpp<HMIR>(t01), wb = t23 + dpp<HMIR>(t23);
  float w = a.b4 ? wb : wa;
  w += dpp<XOR1>(w); w += dpp<XOR2>(w);
  const float sc = rbf(rbf(w) * 0.08838834764831845f);
  if (__builtin_amdgcn_ballot_w64(sc > a.m) != 0) {
    const float mx = fmaxf(a.m, wave_max(sc));
    const float f = __builtin_amdgcn_exp2f((a.m - mx) * 1.4426950408889634f);
    a.acc *= f; a.l *= f; a.m = mx;
  }
  const float p = __builtin_amdgcn_exp2f((sc - a.m) * 1.4426950408889634f);
  a.l += p;
  const float lo = p - __uint_as_float(__float_as_uint(p) & 0xffff0000u);
  const u32 pk = __builtin_amdgcn_perm(__float_as_uint(p), __float_as_uint(lo), 0x07060302u);
  const u32 pu[4] = {dppu<BCAST + 0>(pk), dppu<BCAST + 8>(pk), dppu<BCAST + 4>(pk), dppu<BCAST + 12>(pk)};
#pragma unroll
  for (int u = 0; u < 4; ++u) {
    u32x4 av;
    av.x = __builtin_amdgcn_perm(0u, pu[u], a.sel[0]); av.y = __builtin_amdgcn_perm(0u, pu[u], a.sel[1]);
    av.z = __builtin_amdgcn_perm(0u, pu[u], a.sel[2]); av.w = __builtin_amdgcn_perm(0u, pu[u], a.sel[3]);
    a.acc = mfma(__builtin_bit_cast(bf16x8, av), __builtin_bit_cast(bf16x8, t.v[u]), a.acc);
  }
}

struct LN { float meanb, den, rden; };
__device__ __forceinline__ LN ln_stats(const u32 (&xp)[5]) {
  const float x0 = __uint_as_float((u32)__builtin_amdgcn_readfirstlane((int)(xp[0] << 16)));
  float s = 0.f, v = 0.f;
#pragma unroll
  for (int i = 0; i < 5; ++i) { const float d0 = bf_lo(xp[i]) - x0, d1 = bf_hi(xp[i]) - x0; s += d0 + d1; v += d0 * d0 + d1 * d1; }
  const float md = wave_sum(s) * (1.f / 640.f), mean = x0 + md;
  const float var = rbf(fmaxf(wave_sum(v) * (1.f / 640.f) - md * md, 0.f));
  LN r; r.meanb = rbf(mean); r.den = rbf(sqrtf(rbf(var + 1e-5f)));
  r.rden = __builtin_amdgcn_rcpf(r.den);
  return r;
}
__device__ __forceinline__ float ln_mod(float x, const LN& n, float c1, float sh) {
  const float a = rbf(x - n.meanb), q0 = a * n.rden, q = fmaf(fmaf(-q0, n.den, a), n.rden, q0);
  return rbf(rbf(rbf(q) * c1) + sh);
}

__global__ void __launch_bounds__(512, 2) fused_decode_kernel(
    const u16* __restrict__ x, const u16* __restrict__ kc, const u16* __restrict__ vc, const u16* __restrict__ wq,
    const u16* __restrict__ wo, const u16* __restrict__ wg, const u16* __restrict__ wu, const u16* __restrict__ wd,
    const u16* __restrict__ cnd, const u16* __restrict__ wmod, u16* __restrict__ out, unsigned char* ws) {
  const u32 clk0 = (u32)__builtin_amdgcn_s_memrealtime();
  __shared__ __attribute__((aligned(16))) unsigned char smem_[LDS_BYTES];
  LAS unsigned char* sm = (LAS unsigned char*)smem_;
  const int tid = threadIdx.x, lane = tid & 63;
  const int wave = __builtin_amdgcn_readfirstlane(tid >> 6);
  const int xcd = blockIdx.x & 7, j = blockIdx.x >> 3;
  const int head = 4 * xcd + (j >> 3), split = j & 7;
  asm volatile("" : "+s"(x), "+s"(kc), "+s"(vc), "+s"(wq), "+s"(wo), "+s"(wg));
  asm volatile("" : "+s"(wu), "+s"(wd), "+s"(cnd), "+s"(wmod), "+s"(out), "+s"(ws));
  const bool is_d = xcd >= 3 && j >= 24;
  const int dj = 8 * (xcd - 3) + (j - 24), fj = xcd < 3 ? 32 * xcd + j : 96 + 24 * (xcd - 3) + j;

  constexpr u32 F_C1 = L_FLAG, F_C2 = L_FLAG + 4, F_Q = L_FLAG + 8, F_FREE = L_FLAG + 12, F_HP = L_FLAG + 16;
  const u32 lds0 = (u32)(size_t)sm;
  const u64 wsb = (u64)ws;
  u64 epoch;
  const bool crit = j < 10;
#define EPOCH_LOAD() asm volatile("s_load_dwordx2 %0, %1, 0x0" : "=s"(epoch) : "s"(wsb))
#define EPOCH_WAIT() asm volatile("s_waitcnt lgkmcnt(0)" : "+s"(epoch) :: "memory")
  u64 next; Tag tag;

  const u32 voff = (u32)(wave & 3) * 4096u + (u32)lane * 16u;
  const u64 kb = (u64)kc + ((u64)head * SK + (u64)split * 2048) * 256, vb = (u64)vc + ((u64)head * SK + (u64)split * 2048) * 256;
#define UNDEF(T) asm volatile("" : "=v"(T.k[0]), "=v"(T.k[1]), "=v"(T.k[2]), "=v"(T.k[3]), "=v"(T.v[0]), "=v"(T.v[1]), "=v"(T.v[2]), "=v"(T.v[3]))
  constexpr u64 TS = 16384;
  const u32 clk1 = clk0 + PT0 + (u32)(wave & 3) * PHW + ((((u32)blockIdx.x * 89u) & 255u) * PHB >> 8);
  auto due = [&](u32 n) -> u32 { return clk1 + ((n * PK + (n < PNB ? n : PNB) * PSLOW) >> 4); };
#define DUE(n) until(due(n))
#define ISSUE(R, n) issue(R, voff, kb + (u64)(n) * TS, vb + (u64)(n) * TS)
  if (__builtin_expect(wave < 4, 1)) {
    Tile r0, r1, r2, r3, r4, r5;
    if (tid < 16) lds_st<u32>(sm, L_FLAG + tid * 4, 0u);
    UNDEF(r0);
    ISSUE(r0, 0);
    wg_barrier();
    EPOCH_LOAD();
    Attn a;
    attn_init(a, lane);
    EPOCH_WAIT();
    next = epoch + 1; tag = Tag{(u32)next << 24, (u32)(next >> 8)};
    UNDEF(r1); UNDEF(r2); UNDEF(r3); UNDEF(r4); UNDEF(r5);
    const u32 lt = L_KV + (u32)wave * 8192u, ll_ = lt + (u32)lane * 16u;
#define HD() vmn<8>()
    HD(); DUE(1); ISSUE(r1, 1); HD(); DUE(2); ISSUE(r2, 2); HD(); DUE(3); ISSUE(r3, 3);
    HD(); DUE(4); ISSUE(r4, 4); HD(); DUE(5); ISSUE(r5, 5);
    HD(); DUE(6); issue_lds(voff, kb + 6 * TS, vb + 6 * TS, lds0 + lt);
    HD(); DUE(7); issue_lds(voff, kb + 7 * TS, vb + 7 * TS, lds0 + lt + 32768);
    HD(); DUE(8); issue_lds(voff, kb + 8 * TS, vb + 8 * TS, lds0 + lt + 65536);
    lds_wait(sm, F_Q, 4);
    a.q = lds_ld<u32x4>(sm, L_Q + (lane & 15) * 16);
    wait_but<2>(a); pin(r0); pin(r1); pin(r2); pin(r3); pin(r4); pin(r5);
#define STEP(n, RI, RC) DUE((n) + 2); ISSUE(RI, (n) + 2); wait_but<2>(a); pin(RC); consume(RC, a)
    consume(r0, a); consume(r1, a); consume(r2, a); consume(r3, a); consume(r4, a); consume(r5, a);
    fetch_lds(r2, sm, ll_); consume(r2, a);
    wait_but<1>(a);
    fetch_lds(r2, sm, ll_ + 32768); consume(r2, a);
    wait_but<0>(a);
    fetch_lds(r2, sm, ll_ + 65536);
    lds_arrive(sm, F_FREE, lane);
    consume(r2, a);
    DUE(14); ISSUE(r0, 14); DUE(15); ISSUE(r1, 15);
    for (int n = 14; n < 29; n += 3) {
      if (n == MERGE_AT) {
        lds_wait(sm, F_HP, 4);
        const float mh = lds_ld<float>(sm, L_HM + wave * 4), lh = lds_ld<float>(sm, L_HL + wave * 256 + lane * 4);
        const f32x4 ah = lds_ld<f32x4>(sm, L_HACC + wave * 1024 + lane * 16);
        const float mx = fmaxf(a.m, mh);
        const float f = __builtin_amdgcn_exp2f((a.m - mx) * 1.4426950408889634f), fh = __builtin_amdgcn_exp2f((mh - mx) * 1.4426950408889634f);
        a.acc = a.acc * f + ah * fh; a.l = a.l * f + lh * fh; a.m = mx;
      }
      STEP(n, r2, r0); STEP(n + 1, r0, r1); STEP(n + 2, r1, r2);
    }
    asm volatile(".Lcode_a:");
    STEP(29, r2, r0);
    wait_but<1>(a); pin(r1); consume(r1, a);
    wait_but<0>(a); pin(r2);
    consume(r2, a);
    const float lw = wave_sum(a.l) * 0.25f;
    lds_st<f32x4>(sm, L_ACC + wave * 1024 + lane * 16, a.acc);
    if (lane == 0) lds_st<f32x2>(sm, L_ML + wave * 8, f32x2{a.m, lw});
  } else {
    const int hw = wave - 4;
    __builtin_amdgcn_s_setprio(3);
    const int ll = lane < 40 ? lane : 39;
    const int cjob = crit ? 10 * xcd + j : 0;
    u32x2 cv, cw; u32 xk, xp[5];
    const int p10 = (lane & 15) < 10 ? (lane & 15) : 9;
    const int kq = 80 * split + 20 * hw + 2 * p10;
    int kk[3];
#pragma unroll
    for (int i = 0; i < 3; ++i) kk[i] = 160 * hw + 64 * i + (i < 2 ? lane : (lane & 31));
    const u32 vm_ = (u32)(lane >> 3) * 7680u + (u32)(lane & 7) * 16u, vq_ = (u32)(lane >> 4) * 8192u + (u32)(lane & 15) * 16u;
    auto mod_loads = [&](int job, int i) {
      dma16(vm_, (u64)wmod + (u64)(160 * (job & 3) + 8 * (5 * hw + i)) * 7680 + (job >> 2) * 128, lds0 + L_WMOD + (hw * 5 + i) * 1024);
    };
    auto wq_loads = [&]() {
#pragma unroll
      for (int i = 0; i < 5; ++i) dma16(vq_, (u64)wq + (u64)(80 * split + 4 * (5 * hw + i)) * 8192 + head * 256, lds0 + L_WQ + (hw * 5 + i) * 1024);
    };
    wg_barrier();
    asm volatile("global_load_dwordx2 %0, %1, %2 nt" : "=&v"(cv) : "v"((u32)(160 * (cjob & 3) + 4 * ll) * 2), "s"((u64)cnd) : "memory");
    asm volatile("global_load_dwordx2 %0, %1, %2 nt" : "=&v"(cw) : "v"((u32)(160 * hw + 4 * ll) * 2), "s"((u64)cnd) : "memory");
    if (crit) {
#pragma unroll
      for (int i = 0; i < 5; ++i) mod_loads(cjob, i);
      touch(WS_MP, wsb, lds0 + L_OB + hw * 256);
    }
#pragma unroll
    for (int i = 0; i < 5; ++i) asm volatile("global_load_dword %0, %1, %2 offset:%3 nt" : "=&v"(xp[i]) : "v"((u32)lane * 4), "s"((u64)x), "i"(256 * i) : "memory");
    asm volatile("global_load_dword %0, %1, %2 nt" : "=&v"(xk) : "v"((u32)kq * 2), "s"((u64)x) : "memory");
    if (!crit) wq_loads();
    EPOCH_LOAD();
    EPOCH_WAIT();
    next = epoch + 1; tag = Tag{(u32)next << 24, (u32)(next >> 8)};
    auto mod_gemv = [&](const u32x2& c4, u32 arrivals) -> float {
      lds_st<u32x2>(sm, L_SC + hw * 320 + ll * 8, u32x2{pack_bf(siluf(bf_lo(c4.x)), siluf(bf_hi(c4.x))), pack_bf(siluf(bf_lo(c4.y)), siluf(bf_hi(c4.y)))});
      lds_arrive(sm, F_C1, lane);
      lds_wait(sm, F_C1, arrivals);
      f32x4 acc = {0.f, 0.f, 0.f, 0.f};
#pragma unroll
      for (int i = 0; i < 5; ++i) acc = mfma(a_frag(sm, L_SC + hw * 320 + 64 * i, lane), tr_frag_p<128>(sm, L_WMOD + 32 * i * 128 + 32 * hw, lane), acc);
      return acc.x;
    };
    if (crit) {
      vmn<7>();
      asm volatile("" : "+v"(cv), "+v"(cw));
      const float v = mod_gemv(cv, 4);
      const u32 q = f24(v), qn = dppu<XOR1>(q);
      const int cb = cjob >> 2, ch = cb >= 10 ? 1 : 0;
      vm0();
      if (!(lane & 1)) put2(wsb + WS_MP, (u32)((((lane >> 4) * 2 + ch) * 4 + (cjob & 3)) * 640 + 64 * (cb - 10 * ch) + 16 * hw + (lane & 15)) * 8, q, qn, tag);
      wq_loads();
    } else {
      vmn<5>();
    }
    asm volatile("" : "+v"(cw), "+v"(xp[0]), "+v"(xp[1]), "+v"(xp[2]), "+v"(xp[3]), "+v"(xp[4]), "+v"(xk) :: "memory");
    const LN n1 = ln_stats(xp);
    {
      u32x4 g[2];
      const u32 vo[2] = {(u32)((lane >> 4) * 640 + kq) * 8, (u32)((4 + (lane >> 4)) * 640 + kq) * 8};
      until(clk0 + PMP);
      poll<2>(g, vo, wsb + WS_MP + (u64)(xcd & 3) * (8 * 640 * 8), tag);
      const float sa0 = rbf(xrow_sum(v24(g[0].x))), sa1 = rbf(xrow_sum(v24(g[0].z)));
      const float ca0 = rbf(xrow_sum(v24(g[1].x))), ca1 = rbf(xrow_sum(v24(g[1].z)));
      const u32 h = pack_bf(ln_mod(bf_lo(xk), n1, rbf(1.f + ca0), sa0), ln_mod(bf_hi(xk), n1, rbf(1.f + ca1), sa1));
      if (lane < 10) lds_st<u32>(sm, L_H + (10 * hw + lane) * 4, h);
      lds_arrive(sm, F_C2, lane);
      lds_wait(sm, F_C2, 4);
      f32x4 q0 = {0.f, 0.f, 0.f, 0.f}, q1 = {0.f, 0.f, 0.f, 0.f};
#pragma unroll
      for (int i = 0; i < 5; ++i) {
        const s16x4 af = a_frag16(sm, L_H + 32 * i, lane);
        q0 = mfma16(af, tr_frag16<256>(sm, L_WQ + 16 * i * 256 + 64 * hw, lane), q0);
        q1 = mfma16(af, tr_frag16<256>(sm, L_WQ + 16 * i * 256 + 64 * hw + 32, lane), q1);
      }
      const u32 q = f24((lane & 16) ? q1.x : q0.x), qn = dppu<XOR1>(q);
      const u64 qb = wsb + WS_Q + (u64)head * 8 * 128 * 8;
      if (lane < 32 && !(lane & 1)) put2(qb, (u32)(split * 128 + 32 * hw + lane) * 8, q, qn, tag);
    }
    {
      u32x4 g[2];
      const u32 vo[2] = {(u32)((lane >> 4) * 128 + 32 * hw + 2 * (lane & 15)) * 8, (u32)((4 + (lane >> 4)) * 128 + 32 * hw + 2 * (lane & 15)) * 8};
      until(clk0 + PQP);
      poll<2>(g, vo, wsb + WS_Q + (u64)head * 8 * 128 * 8, tag, 1);
      const u32 q2 = pack_bf(xrow_sum(v24(g[0].x) + v24(g[1].x)), xrow_sum(v24(g[0].z) + v24(g[1].z)));
      if (lane < 16) lds_st<u32>(sm, L_Q + (16 * hw + lane) * 4, q2);
      lds_arrive(sm, F_Q, lane);
    }
    const u32 clkw = clk1 - PT0;
#define WDUE(i) until(clkw + PW0 + (u32)(i) * PW)
#define MDUE(i) until(clkw + PW0 + (u32)(i) * PWM)
#define FDUE(i) until(clkw + PF0 + (u32)(i) * PF)
    {
      Tile t0, t1, t2, t3, t4;
      UNDEF(t0); UNDEF(t1); UNDEF(t2); UNDEF(t3); UNDEF(t4);
      DUE(9); ISSUE(t0, 9);
      vmn<9>(); DUE(10); ISSUE(t1, 10);
      vmn<9>(); DUE(11); ISSUE(t2, 11);
      vmn<9>(); DUE(12); ISSUE(t3, 12);
      vmn<9>(); DUE(13); ISSUE(t4, 13);
      lds_wait(sm, F_Q, 4);
      Attn a;
      attn_init(a, lane);
      a.q = lds_ld<u32x4>(sm, L_Q + (lane & 15) * 16);
      __builtin_amdgcn_s_setprio(0);
      wait_but<2>(a); pin(t0); pin(t1); pin(t2);
      consume(t0, a); consume(t1, a); consume(t2, a);
      wait_but<0>(a); pin(t3); pin(t4);
      consume(t3, a); consume(t4, a);
      lds_st<f32x4>(sm, L_HACC + hw * 1024 + lane * 16, a.acc);
      lds_st<float>(sm, L_HL + hw * 256 + lane * 4, a.l);
      if (lane == 0) lds_st<float>(sm, L_HM + hw * 4, a.m);
      lds_arrive(sm, F_HP, lane);
      __builtin_amdgcn_s_setprio(3);
    }
    lds_wait(sm, F_FREE, 4);
    auto wo_load = [&](int i) {
      const u32 id = 5 * hw + i, c_ = id * 64 + (u32)lane, row = (c_ * 6554u) >> 16;
      dma16(row * 1280 + (c_ - row * 10) * 16, (u64)wo + ((u64)head * 128 * 640 + split * 80) * 2, lds0 + L_WO + id * 1024);
    };
    if (is_d) {
      const int cb = 20 + dj;
#pragma unroll
      for (int i = 0; i < 20; ++i) {
        MDUE(i);
        dma16(vm_, (u64)wmod + (u64)(8 * (20 * hw + i)) * 7680 + cb * 128, lds0 + L_WM + (20 * hw + i) * 1024);
        if (i >= 4 && i < 9) wo_load(i - 4);
      }
      touch(WS_C + (u32)(dj * 217 + 216) * 128, wsb, lds0 + L_OB + 3072 + hw * 256);
      lds_st<u32x2>(sm, L_SC + hw * 320 + ll * 8, u32x2{pack_bf(siluf(bf_lo(cw.x)), siluf(bf_hi(cw.x))), pack_bf(siluf(bf_lo(cw.y)), siluf(bf_hi(cw.y)))});
      vmn<0>();
      lds_arrive(sm, F_C1, lane);
      lds_wait(sm, F_C1, 4);
      f32x4 a0 = {0.f, 0.f, 0.f, 0.f}, a1 = {0.f, 0.f, 0.f, 0.f};
#pragma unroll
      for (int i = 0; i < 20; i += 2) {
        a0 = mfma(a_frag(sm, L_SC + 64 * i, lane), tr_frag_p<128>(sm, L_WM + 32 * i * 128 + 32 * hw, lane), a0);
        a1 = mfma(a_frag(sm, L_SC + 64 * i + 64, lane), tr_frag_p<128>(sm, L_WM + 32 * (i + 1) * 128 + 32 * hw, lane), a1);
      }
      const float v = a0.x + a1.x;
      const int chunk = cb / 10, mcol = 64 * (cb - 10 * chunk) + 16 * hw + (lane & 15);
      const u32 mslot = (u32)(((chunk == 3 || chunk == 4) ? 1 : 2) * 1280 + 2 * mcol + (chunk >= 4 ? 1 : 0));
      const u32 v16 = pack_bf(v, v) & 0xffffu, o = WS_MOD + (u32)(((lane >> 4) * 3840 + mslot) * 8);
      put16(ws, o, v16, tag); put16(ws, o + 4 * 3840 * 8, v16, tag);
    } else {
#pragma unroll
      for (int i = 0; i < 5; ++i) { WDUE(i); wo_load(i); }
#pragma unroll
      for (int i = 0; i < 3; ++i) {
        const int id = hw + 4 * i;
        WDUE(5 + i);
        if (id < 10) dma16((u32)lane * 16, (u64)wd + (u64)fj * 10240 + id * 1024, lds0 + L_WD + id * 1024);
      }
      touch(WS_C + (u32)((lane < 40 ? lane : 39) * 217 + 216) * 128, wsb, lds0 + L_OB + 3072 + hw * 256);
#pragma unroll
      for (int i = 0; i < 5; ++i) { FDUE(i); dma16g((lane & 1 ? wu : wg) + fj * 8 + (size_t)(160 * hw + 32 * i + (lane >> 1)) * FF, lds0 + L_WF + (5 * hw + i) * 1024); }
      vmn<0>();
    }
    if (hw >= 2) {
      u64 pc; int oa, ob;
      asm volatile("s_getpc_b64 %0\n.Lcode_r:\n\ts_mov_b32 %1, .Lcode_a-.Lcode_r\n\ts_mov_b32 %2, .Lcode_b-.Lcode_r" : "=s"(pc), "=s"(oa), "=s"(ob));
      const int lo = oa < ob ? oa : ob, len = (oa < ob ? ob - oa : oa - ob) & ~3;
      const u32 o = (u32)((hw - 2) * 64 + lane) * 128u;
      touch(o < (u32)len ? o : (u32)len, pc + (long long)lo, lds0 + L_OB + 3072 + hw * 256);
    }
    const u64 mrep = wsb + WS_MOD + (u64)xcd * 3840 * 8;
    if (!is_d) {
      u32x4 g[3];
      const u32 vo[3] = {(u32)kk[0] * 16 + 10240, (u32)kk[1] * 16 + 10240, (u32)kk[2] * 16 + 10240};
      poll<3>(g, vo, mrep, tag, 1);
#pragma unroll
      for (int i = 0; i < 3; ++i) {
        lds_st<u16>(sm, L_SB + kk[i] * 2, (u16)g[i].x);
        lds_st<u16>(sm, L_CB1 + kk[i] * 2, (u16)pack_bf(1.f + __uint_as_float(g[i].z << 16), 0.f));
      }
    } else if (hw == 1) {
      const int col = lane & 15;
      u32x4 g[1];
      const u32 vo[1] = {(u32)(20480 + (dj * 16 + col) * 16)};
      u32 xc;
      asm volatile("global_load_ushort %0, %1, %2 nt" : "=&v"(xc) : "v"((u32)(dj * 16 + col) * 2), "s"((u64)x) : "memory");
      poll<1>(g, vo, mrep, tag, 1);
      asm volatile("" : "+v"(xc));
      if (lane < 16) {
        lds_st<float>(sm, L_D16 + 64 + lane * 4, __uint_as_float(g[0].x << 16));
        lds_st<float>(sm, L_D16 + 128 + lane * 4, __uint_as_float(g[0].z << 16));
        lds_st<float>(sm, L_D16 + lane * 4, __uint_as_float(xc << 16));
      }
    }
  }

  wg_barrier();
  const u64 a0blk = wsb + WS_A0 + (u64)head * 8 * 144 * 8;
  if (wave < 2) {
    const f32x2 p0 = lds_ld<f32x2>(sm, L_ML), p1 = lds_ld<f32x2>(sm, L_ML + 8), p2 = lds_ld<f32x2>(sm, L_ML + 16), p3 = lds_ld<f32x2>(sm, L_ML + 24);
    const float M = fmaxf(fmaxf(p0.x, p1.x), fmaxf(p2.x, p3.x));
    const float e0 = __expf(p0.x - M), e1 = __expf(p1.x - M), e2 = __expf(p2.x - M), e3 = __expf(p3.x - M);
    if (wave == 0) {
      const int c = lane >> 2, i = (2 * lane) & 7;
      const u32 o = L_ACC + (u32)(((i >> 2) * 16 + c) * 4 + (i & 3)) * 4;
      const f32x2 v = e0 * (lds_ld<f32x2>(sm, o) + lds_ld<f32x2>(sm, o + 512)) + e1 * (lds_ld<f32x2>(sm, o + 1024) + lds_ld<f32x2>(sm, o + 1536))
                    + e2 * (lds_ld<f32x2>(sm, o + 2048) + lds_ld<f32x2>(sm, o + 2560)) + e3 * (lds_ld<f32x2>(sm, o + 3072) + lds_ld<f32x2>(sm, o + 3584));
      put2(a0blk, (u32)(split * 144 + 2 * lane) * 8, f24(v.x), f24(v.y), tag);
    } else if (lane == 0) {
      put2(a0blk, (u32)(split * 144 + 128) * 8, f24(M), f24(e0 * p0.y + e1 * p1.y + e2 * p2.y + e3 * p3.y), tag);
    }
  }
  bf16x8 bo[4];
  if (wave < 5) {
#pragma unroll
    for (int t = 0; t < 4; ++t) bo[t] = tr_frag_p<160>(sm, L_WO + t * 32 * 160 + wave * 32, lane);
  }
  const u32 vo0[2] = {(u32)(wave * 144 + 2 * lane) * 8, (u32)(wave * 144 + 128) * 8};
  u32x4 g0[2];
  poll<2>(g0, vo0, a0blk, tag);
  lds_st<f32x2>(sm, L_O + (wave * 128 + 2 * lane) * 4, f32x2{v24(g0[0].x), v24(g0[0].z)});
  if (lane == 0) lds_st<f32x2>(sm, L_OML + wave * 8, f32x2{v24(g0[1].x), v24(g0[1].z)});
  wg_barrier();
  const u32 stage = L_OB + (u32)wave * 512;
  if (wave < 5) {
    const f32x2 ml = lds_ld<f32x2>(sm, L_OML + (lane & 7) * 8);
    float M = ml.x;
    M = fmaxf(M, dpp<XOR1>(M)); M = fmaxf(M, dpp<XOR2>(M)); M = fmaxf(M, dpp<HMIR>(M));
    const float e = __expf(ml.x - M);
    float L = e * ml.y;
    L += dpp<XOR1>(L); L += dpp<XOR2>(L); L += dpp<HMIR>(L);
    const float wn = e * __builtin_amdgcn_rcpf(L);
    float o0 = 0.f, o1 = 0.f;
#pragma unroll
    for (int s = 0; s < 8; ++s) {
      const float ws_ = __uint_as_float((u32)__builtin_amdgcn_readlane((int)__float_as_uint(wn), s));
      const f32x2 v = lds_ld<f32x2>(sm, L_O + (s * 128 + 2 * lane) * 4);
      o0 += ws_ * v.x; o1 += ws_ * v.y;
    }
    lds_st<u32>(sm, stage + lane * 4, pack_bf(o0, o1));
    lgkm0();
    f32x4 acc = {0.f, 0.f, 0.f, 0.f};
#pragma unroll
    for (int t = 0; t < 4; ++t) acc = mfma(a_frag(sm, stage + t * 64, lane), bo[t], acc);
    const u32 q = f24(acc.x), qn = dppu<XOR1>(q);
    if (lane < 16 && !(lane & 1)) put2(wsb + WS_A, (u32)(((split * 5 + wave) * 32 + head) * 16 + lane) * 8, q, qn, tag);
  }
  if (!is_d) {
    if (wave < 5) {
      bf16x8 bf[4];
#pragma unroll
      for (int t = 0; t < 4; ++t) bf[t] = tr_frag(sm, L_WF + (wave * 4 + t) * 1024, lane);
      const u32 cb1 = lds_ld<u32>(sm, L_CB1 + (wave * 64 + lane) * 4), sb = lds_ld<u32>(sm, L_SB + (wave * 64 + lane) * 4);
      float wl[8], wh[8];
#pragma unroll
      for (int k = 0; k < 8; ++k) { const u32 p = lds_ld<u32>(sm, L_WD + k * 1280 + (wave * 64 + lane) * 4); wl[k] = bf_lo(p); wh[k] = bf_hi(p); }
      const u32 vo[1] = {(u32)(wave * 64 + lane) * 16};
      u32x4 gx[1];
      poll<1>(gx, vo, wsb + WS_B + (u64)xcd * 640 * 8, tag);
      const u32 own = payload(gx[0]);
      lds_st<u32>(sm, L_X2 + (wave * 64 + lane) * 4, own);
      wg_barrier();
      u32 xp[5];
#pragma unroll
      for (int i = 0; i < 5; ++i) xp[i] = lds_ld<u32>(sm, L_X2 + (i * 64 + lane) * 4);
      const LN n2 = ln_stats(xp);
      lds_st<u32>(sm, stage + lane * 4, pack_bf(ln_mod(bf_lo(own), n2, bf_lo(cb1), bf_lo(sb)), ln_mod(bf_hi(own), n2, bf_hi(cb1), bf_hi(sb))));
      lgkm0();
      f32x4 acc = {0.f, 0.f, 0.f, 0.f};
#pragma unroll
      for (int t = 0; t < 4; ++t) acc = mfma(a_frag(sm, stage + t * 64, lane), bf[t], acc);
      if (lane < 16) lds_st<float>(sm, L_P + (wave * 16 + lane) * 4, acc.x);
      wg_barrier();
      {
        const int col = lane & 15;
        float v = 0.f;
#pragma unroll
        for (int w = 0; w < 5; ++w) v += lds_ld<float>(sm, L_P + (w * 16 + col) * 4);
        v = rbf(v);
        const float u = dpp<ROR8>(v);
        const float act = rbf(rbf(siluf(v)) * u);
        float p0 = 0.f, p1 = 0.f;
#pragma unroll
        for (int k = 0; k < 8; ++k) {
          const float a = __uint_as_float((u32)__builtin_amdgcn_readlane((int)__float_as_uint(act), k));
          p0 += a * wl[k]; p1 += a * wh[k];
        }
        const u32 cp = (u32)(wave * 64 + lane);
        put2(wsb + WS_C, ((cp >> 3) * 217 + (u32)fj) * 128 + (cp & 7) * 16, f24(p0), f24(p1), tag);
      }
    } else {
      wg_barrier();
      wg_barrier();
    }
  } else {
    if (wave == 0) touch((u32)dj * 32, (u64)out, lds0 + L_OB + 3072);
    if (wave == 7) {
      u32 vo[4];
#pragma unroll
      for (int i = 0; i < 4; ++i) vo[i] = (u32)((dj * 32 + 8 * i + (lane >> 3)) * 16 + 2 * (lane & 7)) * 8;
      u32x4 gy[4];
      poll<4>(gy, vo, wsb + WS_A, tag);
      float y0 = 0.f, y1 = 0.f;
#pragma unroll
      for (int i = 0; i < 4; ++i) { y0 += v24(gy[i].x); y1 += v24(gy[i].z); }
      y0 += dpp<ROR8>(y0); y1 += dpp<ROR8>(y1);
      y0 = rbf(xrow_sum(y0)); y1 = rbf(xrow_sum(y1));
      const u32 c2 = (u32)(lane & 7) * 8;
      const f32x2 xx = lds_ld<f32x2>(sm, L_D16 + c2), ga = lds_ld<f32x2>(sm, L_D16 + 64 + c2);
      const float x20 = rbf(xx.x + rbf(ga.x * y0)), x21 = rbf(xx.y + rbf(ga.y * y1));
      put2(wsb + WS_B, (u32)((lane >> 3) * 640 + dj * 16 + 2 * (lane & 7)) * 8, __float_as_uint(x20) >> 16, __float_as_uint(x21) >> 16, tag);
      if (lane < 8) lds_st<f32x2>(sm, L_D16 + 192 + c2, f32x2{x20, x21});
      if (dj == 0 && lane == 0) gst(ws, WS_EPOCH, next);
    }
    wg_barrier();
    u32 vo2[4];
#pragma unroll
    for (int i = 0; i < 4; ++i) { const int f = 8 * i + (lane >> 3); vo2[i] = (u32)((27 * wave + (f < 27 ? f : 26)) * 128 + (lane & 7) * 16); }
    u32x4 gc[4];
    poll<4>(gc, vo2, wsb + WS_C + (u64)dj * 217 * 128, tag);
    float s0 = v24(gc[0].x) + v24(gc[1].x) + v24(gc[2].x), s1 = v24(gc[0].z) + v24(gc[1].z) + v24(gc[2].z);
    if (lane < 24) { s0 += v24(gc[3].x); s1 += v24(gc[3].z); }
    s0 += dpp<ROR8>(s0); s1 += dpp<ROR8>(s1);
    s0 = xrow_sum(s0); s1 = xrow_sum(s1);
    if (lane < 8) lds_st<f32x2>(sm, L_P + (wave * 16 + 2 * lane) * 4, f32x2{s0, s1});
    wg_barrier();
    if (wave == 0) {
      const int col = lane & 15;
      float v = 0.f;
#pragma unroll
      for (int w = 0; w < 8; ++w) v += lds_ld<float>(sm, L_P + (w * 16 + col) * 4);
      const float o = lds_ld<float>(sm, L_D16 + 192 + col * 4) + rbf(lds_ld<float>(sm, L_D16 + 128 + col * 4) * rbf(v));
      const u32 pr = pack_bf(o, dpp<XOR1>(o));
      if (lane < 16 && !(lane & 1)) __hip_atomic_store((GAS u32*)out + dj * 8 + (lane >> 1), pr, RLX_AGENT);
    }
    asm volatile(".Lcode_b:");
  }
}

extern "C" void kernel_launch(void* const* d_in, const int* in_sizes, int n_in, void* d_out, int out_size, void* d_ws, size_t ws_size, hipStream_t stream) {
  fused_decode_kernel<<<256, 512, 0, stream>>>((const u16*)d_in[0], (const u16*)d_in[1], (const u16*)d_in[2], (const u16*)d_in[3], (const u16*)d_in[4],
                                               (const u16*)d_in[6], (const u16*)d_in[7], (const u16*)d_in[8], (const u16*)d_in[9], (const u16*)d_in[10],
                                               (u16*)d_out, (unsigned char*)d_ws);
}
```
